```python
import torch

def _softmax(x, axis=-1):
    return torch.softmax(x, dim=axis)
def _gelu(x):
    return torch.nn.functional.gelu(x, approximate="tanh")
def _silu(x):
    return torch.nn.functional.silu(x)
def _sigmoid(x):
    return torch.sigmoid(x)
def _layernorm(x, axis=-1, eps=1e-5):
    m = x.mean(dim=axis, keepdim=True)
    v = x.var(dim=axis, keepdim=True, unbiased=False)
    return (x - m) / torch.sqrt(v + eps)
def _scatter_add(tgt, idx, src, axis=0):
    return tgt.clone().index_add_(axis, idx, src)
def _segment_sum(src, seg, n):
    out = torch.zeros((n,) + src.shape[1:], dtype=src.dtype)
    return out.index_add_(0, seg, src)
def _pad(x, pw):
    flat = tuple(v for pair in reversed(pw) for v in pair)
    return torch.nn.functional.pad(x, flat)

kernel_name = 'sequential_preLN__linear_kernel_hedgehog__glu_gated_geglu__prompt'

def setup_inputs():

    _w = (lambda a: (lambda t: t.to(torch.bfloat16) if t.is_floating_point() else t)(torch.as_tensor(a)))
    def _A_setup():
        B, S, D, H = 1, 2048, 640, 32
        Dh = D // H
        return {
            "x": _w((lambda *s: torch.randn(*s, dtype=torch.bfloat16) * 0.05)(B, S, D)),
            "wqkv": _w((lambda *s: torch.randn(*s, dtype=torch.bfloat16) * 0.2)(D, 3 * D)),
            "w_phi": _w((lambda *s: torch.randn(*s, dtype=torch.bfloat16) * 0.2)(H, Dh, Dh) * (Dh ** -0.5)),
            "wo": _w((lambda *s: torch.randn(*s, dtype=torch.bfloat16) * 0.05)(D, D)),
        }
    _A_in = _A_setup()

    def _B_setup():
        B, S, Dm, Df = 1, 2048, 640, 1728
        return {
            "x": _w((lambda *s: torch.randn(*s, dtype=torch.bfloat16) * 0.05)(B, S, Dm)),
            "w_gate": _w((lambda *s: torch.randn(*s, dtype=torch.bfloat16) * 0.4)(Dm, Df) * (Dm ** -0.5)),
            "w_up": _w((lambda *s: torch.randn(*s, dtype=torch.bfloat16) * 0.4)(Dm, Df) * (Dm ** -0.5)),
            "w_down": _w((lambda *s: torch.randn(*s, dtype=torch.bfloat16) * 0.4)(Df, Dm) * (Df ** -0.5)),
        }
    _B_in = _B_setup()

    return {
        "A_x": _A_in["x"],
        "A_wqkv": _A_in["wqkv"],
        "A_w_phi": _A_in["w_phi"],
        "A_wo": _A_in["wo"],
        "B_x": _B_in["x"],
        "B_w_gate": _B_in["w_gate"],
        "B_w_up": _B_in["w_up"],
        "B_w_down": _B_in["w_down"],
    }

def reference(A_x, A_wqkv, A_w_phi, A_wo, B_x, B_w_gate, B_w_up, B_w_down):
    def _A_ref(x, wqkv, w_phi, wo):
        B, S, D = x.shape
        H = 32; Dh = D // H
        qkv = (lambda x, *d: x.permute(*d))((x @ wqkv).reshape(B, S, 3, H, Dh), 2, 0, 3, 1, 4)
        q, k, v = qkv[0], qkv[1], qkv[2]
        pq = torch.einsum("bhsd,hdf->bhsf", q, w_phi)
        pk = torch.einsum("bhsd,hdf->bhsf", k, w_phi)
        phi_q = torch.concatenate([_softmax(pq, axis=-1), _softmax(-pq, axis=-1)], axis=-1)
        phi_k = torch.concatenate([_softmax(pk, axis=-1), _softmax(-pk, axis=-1)], axis=-1)
        kv = torch.einsum("bhsf,bhsd->bhfd", phi_k, v)
        z  = torch.einsum("bhsf->bhf", phi_k)
        num = torch.einsum("bhqf,bhfd->bhqd", phi_q, kv)
        den = torch.einsum("bhqf,bhf->bhq", phi_q, z)[..., None] + 1e-6
        o = num / den
        return (lambda x, *d: x.permute(*d))(o, 0, 2, 1, 3).reshape(B, S, D) @ wo

    def _B_ref(x, w_gate, w_up, w_down):
        g = torch.einsum("bsd,df->bsf", x, w_gate)
        u = torch.einsum("bsd,df->bsf", x, w_up)
        h = _gelu(g) * u
        return torch.einsum("bsf,fd->bsd", h, w_down)

    x = A_x
    h = x + _A_ref(_layernorm(x, axis=-1), A_wqkv, A_w_phi, A_wo)
    h2 = h + _B_ref(_layernorm(h, axis=-1), B_w_gate, B_w_up, B_w_down)
    return h2
```

```cpp
#include <hip/hip_runtime.h>
#include <hip/hip_bf16.h>
#include <stdint.h>

typedef unsigned short u16;
static constexpr int S = 2048, D = 640, H = 32, DH = 20, F = 1728;

__device__ __forceinline__ float bf2f(u16 v) { return __uint_as_float(((unsigned)v) << 16); }
__device__ __forceinline__ u16 f2bf(float f) { return __builtin_bit_cast(u16, (__bf16)f); }

__global__ void k_ln(const u16* x, u16* y) {
    int row = blockIdx.x, t = threadIdx.x;
    __shared__ float red[64];
    float v[10]; float s = 0;
    for (int i = 0; i < 10; i++) { v[i] = bf2f(x[row * D + t + 64 * i]); s += v[i]; }
    for (int o = 32; o; o >>= 1) s += __shfl_xor(s, o);
    float m = s / D; float q = 0;
    for (int i = 0; i < 10; i++) { float d = v[i] - m; q += d * d; }
    for (int o = 32; o; o >>= 1) q += __shfl_xor(q, o);
    float r = rsqrtf(q / D + 1e-5f);
    for (int i = 0; i < 10; i++) y[row * D + t + 64 * i] = f2bf((v[i] - m) * r);
}

template <int MODE>
__global__ void k_gemm(const u16* A, const u16* B, const u16* B2, const u16* R, u16* C, int M, int N, int K) {
    __shared__ float As[16][17], Bs[16][17], Bs2[16][17];
    int tx = threadIdx.x & 15, ty = threadIdx.x >> 4;
    int row = blockIdx.y * 16 + ty, col = blockIdx.x * 16 + tx;
    float acc = 0, acc2 = 0;
    for (int k0 = 0; k0 < K; k0 += 16) {
        As[ty][tx] = bf2f(A[row * K + k0 + tx]);
        Bs[ty][tx] = bf2f(B[(k0 + ty) * N + col]);
        if (MODE == 2) Bs2[ty][tx] = bf2f(B2[(k0 + ty) * N + col]);
        __syncthreads();
        for (int k = 0; k < 16; k++) { acc += As[ty][k] * Bs[k][tx]; if (MODE == 2) acc2 += As[ty][k] * Bs2[k][tx]; }
        __syncthreads();
    }
    if (MODE == 0) C[row * N + col] = f2bf(acc);
    if (MODE == 1) C[row * N + col] = f2bf(acc + bf2f(R[row * N + col]));
    if (MODE == 2) {
        float g = acc;
        float t = tanhf(0.7978845608028654f * (g + 0.044715f * g * g * g));
        C[row * N + col] = f2bf(0.5f * g * (1.f + t) * acc2);
    }
}

__global__ void k_feat(const u16* qkv, const u16* wphi, float* phiq, float* phik) {
    int idx = blockIdx.x * blockDim.x + threadIdx.x;
    int which = idx & 1, h = (idx >> 1) & 31, s = idx >> 6;
    const u16* src = qkv + s * 3 * D + which * D + h * DH;
    float p[DH];
#pragma unroll
    for (int f = 0; f < DH; f++) p[f] = 0;
    for (int d = 0; d < DH; d++) {
        float q = bf2f(src[d]);
#pragma unroll
        for (int f = 0; f < DH; f++) p[f] += q * bf2f(wphi[h * DH * DH + d * DH + f]);
    }
    float mx = p[0], mn = p[0];
#pragma unroll
    for (int f = 1; f < DH; f++) { mx = fmaxf(mx, p[f]); mn = fminf(mn, p[f]); }
    float sp = 0, sn = 0;
    float* dst = (which ? phik : phiq) + (s * H + h) * 2 * DH;
#pragma unroll
    for (int f = 0; f < DH; f++) { sp += __expf(p[f] - mx); sn += __expf(mn - p[f]); }
#pragma unroll
    for (int f = 0; f < DH; f++) { dst[f] = __expf(p[f] - mx) / sp; dst[DH + f] = __expf(mn - p[f]) / sn; }
}

__global__ void k_kv(const u16* qkv, const float* phik, float* kv) {
    int h = blockIdx.x, t = threadIdx.x;
    if (t >= 40 * 21) return;
    int f = t / 21, d = t % 21;
    float acc = 0;
    for (int s = 0; s < S; s++) {
        float v = d < 20 ? bf2f(qkv[s * 3 * D + 2 * D + h * DH + d]) : 1.f;
        acc += phik[(s * H + h) * 40 + f] * v;
    }
    kv[h * 840 + t] = acc;
}

__global__ void k_o(const float* phiq, const float* kv, u16* o) {
    int idx = blockIdx.x * blockDim.x + threadIdx.x;
    int c = idx % D, s = idx / D;
    int h = c / DH, d = c % DH;
    float num = 0, den = 0;
    for (int f = 0; f < 40; f++) {
        float p = phiq[(s * H + h) * 40 + f];
        num += p * kv[h * 840 + f * 21 + d];
        den += p * kv[h * 840 + f * 21 + 20];
    }
    o[idx] = f2bf(num / (den + 1e-6f));
}

extern "C" void kernel_launch(void* const* d_in, const int* in_sizes, int n_in,
                              void* d_out, int out_size, void* d_ws, size_t ws_size,
                              hipStream_t stream) {
    const u16* x = (const u16*)d_in[0];
    const u16* wqkv = (const u16*)d_in[1];
    const u16* wphi = (const u16*)d_in[2];
    const u16* wo = (const u16*)d_in[3];
    const u16* wg = (const u16*)d_in[5];
    const u16* wu = (const u16*)d_in[6];
    const u16* wd = (const u16*)d_in[7];
    char* ws = (char*)d_ws;
    u16* xn = (u16*)ws; ws += S * D * 2;
    u16* qkv = (u16*)ws; ws += S * 3 * D * 2;
    float* phiq = (float*)ws; ws += S * H * 40 * 4;
    float* phik = (float*)ws; ws += S * H * 40 * 4;
    float* kv = (float*)ws; ws += H * 840 * 4;
    u16* o = (u16*)ws; ws += S * D * 2;
    u16* h = (u16*)ws; ws += S * D * 2;
    u16* hn = (u16*)ws; ws += S * D * 2;
    u16* hh = (u16*)ws; ws += S * F * 2;
    k_ln<<<S, 64, 0, stream>>>(x, xn);
    k_gemm<0><<<dim3(3 * D / 16, S / 16), 256, 0, stream>>>(xn, wqkv, nullptr, nullptr, qkv, S, 3 * D, D);
    k_feat<<<S * 64 / 256, 256, 0, stream>>>(qkv, wphi, phiq, phik);
    k_kv<<<H, 896, 0, stream>>>(qkv, phik, kv);
    k_o<<<S * D / 256, 256, 0, stream>>>(phiq, kv, o);
    k_gemm<1><<<dim3(D / 16, S / 16), 256, 0, stream>>>(o, wo, nullptr, x, h, S, D, D);
    k_ln<<<S, 64, 0, stream>>>(h, hn);
    k_gemm<2><<<dim3(F / 16, S / 16), 256, 0, stream>>>(hn, wg, wu, nullptr, hh, S, F, D);
    k_gemm<1><<<dim3(D / 16, S / 16), 256, 0, stream>>>(hh, wd, nullptr, h, (u16*)d_out, S, D, F);
}
```

```cpp
#include <hip/hip_runtime.h>
#include <stdint.h>

typedef unsigned short u16;
typedef unsigned long long u64;
typedef unsigned v4u __attribute__((ext_vector_type(4)));
typedef unsigned v2u __attribute__((ext_vector_type(2)));
typedef float f32x2 __attribute__((ext_vector_type(2)));
typedef float f32x4 __attribute__((ext_vector_type(4)));
typedef float f32x16 __attribute__((ext_vector_type(16)));
typedef __bf16 bf16x2 __attribute__((ext_vector_type(2)));
typedef __bf16 bf16x8 __attribute__((ext_vector_type(8)));
typedef short s16x4 __attribute__((ext_vector_type(4)));
typedef __amdgpu_buffer_rsrc_t rsrc_t;
#define GAS __attribute__((address_space(1)))
#define LAS __attribute__((address_space(3)))
#define DI __device__ __forceinline__

namespace {

constexpr int SEQ = 2048, DM = 640, FF = 1728;

constexpr unsigned OFF_EPOCH = 0;
constexpr unsigned OFF_F0R = 1024;
constexpr unsigned OFF_F0C = OFF_F0R + 2048;
constexpr unsigned OFF_FA  = OFF_F0C + 2048;
constexpr unsigned OFF_FB  = OFF_FA + 2048;
constexpr unsigned OFF_FC  = OFF_FB + 2048;
constexpr unsigned OFF_FD  = OFF_FC + 2048;
constexpr unsigned OFF_FWC = OFF_FD + 16384;
constexpr unsigned OFF_FWJ = OFF_FWC + 2048;
constexpr unsigned OFF_XN  = 65536;
constexpr unsigned OFF_W1  = OFF_XN + 64 * 40 * 1024;
constexpr unsigned OFF_KVP = OFF_W1 + 16 * 40 * 4096;
constexpr unsigned OFF_O   = OFF_KVP + 256 * 3584;
constexpr unsigned OFF_W2  = OFF_O + 128 * 20480;
constexpr unsigned OFF_HB  = OFF_W2 + 8 * 100 * 1024;
constexpr unsigned OFF_ST  = OFF_HB + 128 * 20480;
constexpr unsigned OFF_CSP = OFF_ST + 128 * 8 * 128;
constexpr unsigned OFF_W3  = OFF_CSP + 32 * 8 * 512;
constexpr unsigned OFF_HH  = OFF_W3 + 31 * 140 * 1024;
constexpr unsigned OFF_W4  = OFF_HH + 128 * 216 * 256;
constexpr unsigned WS_USED = OFF_W4 + 8 * 270 * 1024;
constexpr unsigned OFF_STAMP = (WS_USED + 4095) & ~4095u;
constexpr unsigned OOB = 0x7ffffff0u;

constexpr int LDS_BYTES = 163840;
constexpr int L_LN   = 0;
constexpr int L_PF   = 65536;
constexpr int L_RAW  = 122880;
constexpr int L_CSC  = L_RAW + 34816;
constexpr int L_W1Q  = 122880;
constexpr int L_PKV  = 0;
constexpr int L_KVT  = 36864;
constexpr int L_W2   = 40960;
constexpr int L_CS   = 143360 + 14336;
constexpr int L_ZR   = L_CS + 512;
__host__ __device__ constexpr int w3slot(int i) { return i < 2 ? 143360 + i * 7168 : (i < 7 ? (i - 2) * 7168 : 40960 + (i - 7) * 7168); }

constexpr int AUX_SC1 = 16;
constexpr float LOG2E = 1.4426950408889634f;

DI rsrc_t mk_rsrc(const void* p, unsigned bytes) { return __builtin_amdgcn_make_buffer_rsrc((void*)p, 0, bytes, 0x00020000); }
DI v4u ld128c(rsrc_t r, unsigned v, unsigned s) { return __builtin_amdgcn_raw_buffer_load_b128(r, v, s, AUX_SC1); }
DI v2u ld64c(rsrc_t r, unsigned v, unsigned s) { return __builtin_amdgcn_raw_buffer_load_b64(r, v, s, AUX_SC1); }
DI unsigned ld32c(rsrc_t r, unsigned v, unsigned s) { return __builtin_amdgcn_raw_buffer_load_b32(r, v, s, AUX_SC1); }
DI v4u ld128p(rsrc_t r, unsigned v, unsigned s) { return __builtin_amdgcn_raw_buffer_load_b128(r, v, s, 0); }
DI v2u ld64p(rsrc_t r, unsigned v, unsigned s) { return __builtin_amdgcn_raw_buffer_load_b64(r, v, s, 0); }
DI unsigned ld16p(rsrc_t r, unsigned v, unsigned s) { return __builtin_amdgcn_raw_buffer_load_b16(r, v, s, 0); }
DI void st128c(v4u d, rsrc_t r, unsigned v, unsigned s) { __builtin_amdgcn_raw_buffer_store_b128(d, r, v, s, AUX_SC1); }
DI void st64c(v2u d, rsrc_t r, unsigned v, unsigned s) { __builtin_amdgcn_raw_buffer_store_b64(d, r, v, s, AUX_SC1); }
DI void st32c(unsigned d, rsrc_t r, unsigned v, unsigned s) { __builtin_amdgcn_raw_buffer_store_b32(d, r, v, s, AUX_SC1); }

DI void dma_c(rsrc_t r, unsigned lds, unsigned v, unsigned s) {
    asm volatile("s_mov_b32 m0, %0\n\ts_nop 0\n\tbuffer_load_dwordx4 %1, %2, %3 offen sc1 lds" ::"s"(lds), "v"(v), "s"(r), "s"(s) : "memory");
}
DI void dma_p(rsrc_t r, unsigned lds, unsigned v, unsigned s) {
    asm volatile("s_mov_b32 m0, %0\n\ts_nop 0\n\tbuffer_load_dwordx4 %1, %2, %3 offen lds" ::"s"(lds), "v"(v), "s"(r), "s"(s) : "memory");
}
template <int N> DI void wait_vm() { asm volatile("s_waitcnt vmcnt(%0)" ::"n"(N) : "memory"); }
DI void bar() { asm volatile("s_barrier" ::: "memory"); }
DI void bar_lds() { asm volatile("s_waitcnt lgkmcnt(0)\n\ts_barrier" ::: "memory"); }

DI v4u lds128(int off) { return *(LAS v4u*)(size_t)(unsigned)off; }
DI void lds_st128(int off, v4u d) { *(LAS v4u*)(size_t)(unsigned)off = d; }
DI void lds_st64(int off, v2u d) { *(LAS v2u*)(size_t)(unsigned)off = d; }
DI v2u lds_tr(int off) { return __builtin_bit_cast(v2u, __builtin_amdgcn_ds_read_tr16_b64_v4i16((LAS s16x4*)(size_t)(unsigned)off)); }

DI f32x16 mfma32(v4u a, v4u b, f32x16 c) { return __builtin_amdgcn_mfma_f32_32x32x16_bf16(__builtin_bit_cast(bf16x8, a), __builtin_bit_cast(bf16x8, b), c, 0, 0, 0); }
DI f32x4 mfma16(v4u a, v4u b, f32x4 c) { return __builtin_amdgcn_mfma_f32_16x16x32_bf16(__builtin_bit_cast(bf16x8, a), __builtin_bit_cast(bf16x8, b), c, 0, 0, 0); }
DI unsigned pk2(float lo, float hi) { f32x2 p = {lo, hi}; bf16x2 q = __builtin_convertvector(p, bf16x2); return __builtin_bit_cast(unsigned, q); }
DI float bflo(unsigned u) { return __uint_as_float(u << 16); }
DI float bfhi(unsigned u) { return __uint_as_float(u & 0xffff0000u); }
DI float dot2(unsigned a, unsigned b, float c) { return __builtin_amdgcn_fdot2_f32_bf16(__builtin_bit_cast(bf16x2, a), __builtin_bit_cast(bf16x2, b), c, false); }
DI float sum_x16(float v) { unsigned u = __float_as_uint(v); auto r = __builtin_amdgcn_permlane16_swap(u, u, false, false); unsigned a = r[0], b = r[1]; return __uint_as_float(a) + __uint_as_float(b); }
DI float sum_x32(float v) { unsigned u = __float_as_uint(v); auto r = __builtin_amdgcn_permlane32_swap(u, u, false, false); unsigned a = r[0], b = r[1]; return __uint_as_float(a) + __uint_as_float(b); }
DI float sum_x8(float v) { return v + __builtin_amdgcn_update_dpp(0.f, v, 0x128, 0xf, 0xf, false); }
DI void swap16(unsigned& a, unsigned& b) { auto r = __builtin_amdgcn_permlane16_swap(a, b, false, false); a = r[0]; b = r[1]; }
DI void swap32(unsigned& a, unsigned& b) { auto r = __builtin_amdgcn_permlane32_swap(a, b, false, false); a = r[0]; b = r[1]; }
DI void swap32f(float& a, float& b) { unsigned x = __float_as_uint(a), y = __float_as_uint(b); swap32(x, y); a = __uint_as_float(x); b = __uint_as_float(y); }

DI void set_flag(char* ws, unsigned off, u64 tag) { __hip_atomic_store((GAS u64*)(ws + off), tag, __ATOMIC_RELAXED, __HIP_MEMORY_SCOPE_AGENT); }
DI void poll(char* ws, unsigned off, u64 tag) {
    GAS u64* p = (GAS u64*)(ws + off);
    for (int sp = 0; sp < (1 << 17); ++sp) {
        u64 v = __hip_atomic_load(p, __ATOMIC_RELAXED, __HIP_MEMORY_SCOPE_AGENT);
        if (__all(v == tag)) break;
    }
    __builtin_amdgcn_fence(__ATOMIC_ACQUIRE, "wavefront");
}

DI void feature_map(const float (&p)[20], unsigned (&o)[20]) {
    float mx = p[0], mn = p[0];
#pragma unroll
    for (int i = 1; i < 20; ++i) { mx = fmaxf(mx, p[i]); mn = fminf(mn, p[i]); }
    float ep[20], en[20], sp = 0.f, sn = 0.f;
#pragma unroll
    for (int i = 0; i < 20; ++i) { ep[i] = __builtin_amdgcn_exp2f(p[i] - mx); en[i] = __builtin_amdgcn_exp2f(mn - p[i]); sp += ep[i]; sn += en[i]; }
    const float rp = __builtin_amdgcn_rcpf(sp), rn = __builtin_amdgcn_rcpf(sn);
#pragma unroll
    for (int i = 0; i < 10; ++i) { o[i] = pk2(ep[2 * i] * rp, ep[2 * i + 1] * rp); o[10 + i] = pk2(en[2 * i] * rn, en[2 * i + 1] * rn); }
}

DI float geglu(float g, float u) {
    const float k1 = -2.f * 0.7978845608028654f * LOG2E, k2 = k1 * 0.044715f;
    const float e = __builtin_amdgcn_exp2f(g * __builtin_fmaf(g * g, k2, k1));
    return g * u * __builtin_amdgcn_rcpf(1.f + e);
}

#define STAMP(i) do {} while (0)
#define CSTAMP(i) do {} while (0)

__global__ __launch_bounds__(512, 2) void fused_block(const u16* __restrict__ x, const u16* __restrict__ wqkv, const u16* __restrict__ wphi,
                                                      const u16* __restrict__ wo, const u16* __restrict__ wg, const u16* __restrict__ wu,
                                                      const u16* __restrict__ wd, u16* __restrict__ out, char* __restrict__ ws) {
    __shared__ __attribute__((aligned(16))) char smem[LDS_BYTES];
    asm volatile("" ::"v"((LAS char*)smem));
    STAMP(0); CSTAMP(22);
    const int tid = threadIdx.x, lane = tid & 63;
    const int wv = __builtin_amdgcn_readfirstlane(tid >> 6);
    const int b = blockIdx.x, xcd = b & 7, rk = b >> 3;
    const int m = (xcd >> 2) * 8 + (rk >> 2), hp = (xcd & 3) * 4 + (rk & 3);
    const int t = xcd * 4 + (rk >> 3), c = rk & 7;
    const int TT = (xcd >> 1) * 2 + (rk >> 4), j = (xcd & 1) * 16 + (rk & 15);
    const rsrc_t rws = mk_rsrc(ws, WS_USED);
    const int l15 = lane & 15, lg = lane >> 4, l31 = lane & 31, lh = lane >> 5;
    const unsigned l16 = lane * 16;

    const v2u ep = ld64c(rws, OFF_EPOCH, 0);
    v4u xa, xb, fa0; v2u fa1; unsigned pb[12];
    const int ftask = m * 8 + wv, fkb = ftask / 6, fty = (ftask % 6) >> 1, fhead = ftask & 1;
    {
        const rsrc_t rx = mk_rsrc(x, SEQ * DM * 2), rq = mk_rsrc(wqkv, DM * 3 * DM * 2), rp = mk_rsrc(wphi, 32 * 400 * 2);
        const unsigned xrow = (128 * m + 8 * hp + (lane & 7)) * 1280;
        xa = ld128p(rx, xrow + (8 * wv + (lane >> 3)) * 16, 0);
        xb = ld128p(rx, lane < 16 ? xrow + (64 + 2 * wv + (lane >> 3)) * 16 : OOB, 0);
        const int r = (l31 & 3) + 4 * (l31 >> 3), krow = 32 * fkb + 16 * (r >> 3) + 8 * ((l31 >> 2) & 1) + (r & 7);
        const int col = (fty == 0 ? 640 : fty == 1 ? 1280 : 0) + (2 * hp + fhead) * 20;
        const unsigned wq = ftask < 120 ? (krow * 1920 + col) * 2 : OOB;
        fa0 = ld128p(rq, wq + 16 * lh, 0);
        fa1 = ld64p(rq, lh == 0 ? wq + 32 : OOB, 0);
        const unsigned po = l31 < 20 ? ((2 * hp + fhead) * 400 + 8 * lh * 20 + l31) * 2 : OOB;
#pragma unroll
        for (int i = 0; i < 8; ++i) pb[i] = ld16p(rp, po + i * 40, 0);
#pragma unroll
        for (int i = 0; i < 4; ++i) pb[8 + i] = ld16p(rp, lh == 0 ? po + (16 + i) * 40 : OOB, 0);
    }
    {
        u64 pc; unsigned len;
        asm volatile("s_getpc_b64 %0\nfb_code_lo:\n\ts_mov_b32 %1, fb_code_hi-fb_code_lo" : "=s"(pc), "=s"(len));
        const rsrc_t rc = mk_rsrc((const void*)pc, len);
        asm volatile("s_mov_b32 m0, %0\n\ts_nop 0\n\tbuffer_load_dword %1, %2, 0 offen lds" ::"s"(L_PF + wv * 256), "v"(tid * 128), "s"(rc) : "memory");
    }
    {
        float s = 0.f, q = 0.f;
        const unsigned one2 = 0x3f803f80u;
#pragma unroll
        for (int i = 0; i < 4; ++i) { unsigned a = xa[i], d = xb[i]; s = dot2(a, one2, s); q = dot2(a, a, q); s = dot2(d, one2, s); q = dot2(d, d, q); }
        s = sum_x32(sum_x16(sum_x8(s))); q = sum_x32(sum_x16(sum_x8(q)));
        if (lane < 8) lds_st64(L_LN + lane * 64 + wv * 8, v2u{__float_as_uint(s), __float_as_uint(q)});
    }
    {
        v4u b0, b1;
        if (fty != 1) {
            b0 = v4u{pb[0] | (pb[1] << 16), pb[2] | (pb[3] << 16), pb[4] | (pb[5] << 16), pb[6] | (pb[7] << 16)};
            b1 = v4u{pb[8] | (pb[9] << 16), pb[10] | (pb[11] << 16), 0u, 0u};
        } else {
            const int e0 = l31 - 8 * lh, e1 = lh == 0 ? l31 - 16 : -1;
#pragma unroll
            for (int i = 0; i < 4; ++i) {
                b0[i] = (e0 == 2 * i ? 0x3f80u : 0u) | (e0 == 2 * i + 1 ? 0x3f800000u : 0u);
                b1[i] = (e1 == 2 * i ? 0x3f80u : 0u) | (e1 == 2 * i + 1 ? 0x3f800000u : 0u);
            }
        }
        f32x16 d = {};
        d = mfma32(fa0, b0, d);
        d = mfma32(v4u{fa1.x, fa1.y, 0u, 0u}, b1, d);
        const float sc = fty == 1 ? 1.f : LOG2E;
        const v4u c0 = {pk2(d[0] * sc, d[1] * sc), pk2(d[2] * sc, d[3] * sc), pk2(d[4] * sc, d[5] * sc), pk2(d[6] * sc, d[7] * sc)};
        const v4u c1 = {pk2(d[8] * sc, d[9] * sc), pk2(d[10] * sc, d[11] * sc), pk2(d[12] * sc, d[13] * sc), pk2(d[14] * sc, d[15] * sc)};
        const unsigned o = (ftask < 120 && l31 < 20) ? OFF_W1 + (hp * 40 + 2 * fkb) * 4096 + lh * 2048 + fhead * 1024 + (fty * 20 + l31) * 16 : OOB;
        st128c(c0, rws, o, 0);
        st128c(c1, rws, o, 4096);
    }
    bar_lds();
    {
        float s = 0.f, q = 0.f;
#pragma unroll
        for (int i = 0; i < 4; ++i) {
            const v4u v = lds128(L_LN + (lane & 7) * 64 + i * 16);
            s += __uint_as_float(v.x) + __uint_as_float(v.z); q += __uint_as_float(v.y) + __uint_as_float(v.w);
        }
        const float mean = s * (1.f / 640.f), var = fmaxf(q * (1.f / 640.f) - mean * mean, 0.f);
        const float rs = __builtin_amdgcn_rsqf(var + 1e-5f), nb = -mean * rs;
        v4u ya, yb;
#pragma unroll
        for (int i = 0; i < 4; ++i) {
            unsigned a = xa[i], d = xb[i];
            ya[i] = pk2(__builtin_fmaf(bflo(a), rs, nb), __builtin_fmaf(bfhi(a), rs, nb));
            yb[i] = pk2(__builtin_fmaf(bflo(d), rs, nb), __builtin_fmaf(bfhi(d), rs, nb));
        }
        const int tok = 128 * m + 8 * hp + (lane & 7);
        const unsigned o = OFF_XN + (tok >> 5) * 40960 + (tok & 31) * 16;
        st128c(ya, rws, o + (8 * wv + (lane >> 3)) * 512, 0);
        st128c(yb, rws, lane < 16 ? o + (64 + 2 * wv + (lane >> 3)) * 512 : OOB, 0);
    }
    const u64 tag = (((u64)__builtin_amdgcn_readfirstlane(ep.y) << 32) | __builtin_amdgcn_readfirstlane(ep.x)) + 1;
    STAMP(1);
    if (wv >= 4) {
        const int hw = wv - 4;
        const u16* src = hw == 0 ? wg : hw == 1 ? wu : hw == 2 ? wd : wo;
        const rsrc_t rr = mk_rsrc(src, hw == 3 ? DM * DM * 2 : DM * FF * 2);
        const int cpr = hw < 2 ? 7 : 10, nrow = hw < 2 ? 80 : hw == 2 ? 56 : 40;
        const unsigned stride = hw < 2 ? FF * 2 : DM * 2;
        const unsigned base = hw < 2 ? (80 * TT * FF + 56 * j) * 2 : hw == 2 ? (56 * t * DM + 80 * c) * 2 : (40 * t * DM + 80 * c) * 2;
        const bool on = hw < 2 ? j < 31 : hw == 2 ? t < 31 : t < 16;
#pragma unroll
        for (int i = 0; i < 9; ++i) {
            const int e = i * 64 + lane, row = hw < 2 ? e / 7 : e / 10, cc = e - row * cpr;
            dma_p(rr, L_RAW + hw * 9216 + i * 1024, (on && row < nrow) ? base + row * stride + cc * 16 : OOB, 0);
        }
        wait_vm<9>();
    } else {
        wait_vm<0>();
    }
    bar();
    STAMP(2);
    if (wv == 0) {
        if (lane < 2) set_flag(ws, lane == 0 ? OFF_F0R + (m * 16 + hp) * 8 : OFF_F0C + (hp * 16 + m) * 8, tag);
        poll(ws, (lane & 16) ? OFF_F0C + (hp * 16 + l15) * 8 : OFF_F0R + (m * 16 + l15) * 8, tag);
    }
    bar();
    STAMP(3);

    const int rot1 = 4 * (((m & 7) + 2 * (hp & 3)) % 10);
    unsigned phq[20];
    v4u xr0, xr1; v2u xr2;
    if (wv < 4) {
        v4u a[40];
        f32x16 acc[4] = {};
        const unsigned xo = OFF_XN + (4 * m + wv) * 40960;
        auto ksrc = [&](int i) { int s = i + rot1; return s >= 40 ? s - 40 : s; };
        constexpr int PD = 12;
#pragma unroll
        for (int i = 0; i < PD; ++i) a[i] = ld128c(rws, l16, xo + ksrc(i) * 1024);
        bar();
        v4u w[2][3];
#pragma unroll
        for (int u = 0; u < 3; ++u) w[0][u] = lds128(l16 + u * 1024);
#pragma unroll
        for (int i = 0; i < 40; ++i) {
            if (i + PD < 40) a[i + PD] = ld128c(rws, l16, xo + ksrc(i + PD) * 1024);
            if (i + 1 < 40) {
                if ((i + 1) % 4 == 0) bar();
#pragma unroll
                for (int u = 0; u < 3; ++u) w[(i + 1) & 1][u] = lds128(l16 + ((i + 1) * 3 + u) * 1024);
            }
#pragma unroll
            for (int u = 0; u < 3; ++u) acc[u] = mfma32(w[i & 1][u], a[i], acc[u]);
            __builtin_amdgcn_sched_barrier(0);
        }
        STAMP(4);
        bar();
        {
            float p[20]; unsigned pk[20];
#pragma unroll
            for (int i = 0; i < 20; ++i) p[i] = acc[i >> 4][i & 15];
            feature_map(p, pk);
            const int ro = L_PKV + (lh * 128 + 32 * wv + l31) * 144;
#pragma unroll
            for (int i = 0; i < 5; ++i) lds_st128(ro + i * 16, v4u{pk[4 * i], pk[4 * i + 1], pk[4 * i + 2], pk[4 * i + 3]});
            unsigned vv[10];
#pragma unroll
            for (int i = 0; i < 10; ++i) vv[i] = pk2(acc[(20 + 2 * i) >> 4][(20 + 2 * i) & 15], acc[(21 + 2 * i) >> 4][(21 + 2 * i) & 15]);
            lds_st128(ro + 80, v4u{vv[0], vv[1], vv[2], vv[3]});
            lds_st128(ro + 96, v4u{vv[4], vv[5], vv[6], vv[7]});
            lds_st128(ro + 112, v4u{vv[8], vv[9], 0x3f80u, 0u});
        }
        bar_lds();
        {
            const int head = wv >> 1, ft = wv & 1, blk = lg & 1, q = l15 >> 2, p = lane & 3;
            const int rb = L_PKV + (head * 128 + 8 * lh + q) * 144;
            const int ao = rb + (32 * ft + 16 * blk + 8 * (p & 1) + 4 * (p >> 1)) * 2, bo = rb + (40 + 16 * blk + 4 * p) * 2;
            f32x16 d = {};
#pragma unroll
            for (int s = 0; s < 8; ++s) {
                const v2u a0 = lds_tr(ao + s * 2304), a1 = lds_tr(ao + s * 2304 + 576), b0 = lds_tr(bo + s * 2304), b1 = lds_tr(bo + s * 2304 + 576);
                d = mfma32(v4u{a0.x, a0.y, a1.x, a1.y}, v4u{b0.x, b0.y, b1.x, b1.y}, d);
            }
            const v4u c0 = {pk2(d[0], d[1]), pk2(d[2], d[3]), pk2(d[4], d[5]), pk2(d[6], d[7])};
            const v4u c1 = {pk2(d[8], d[9]), pk2(d[10], d[11]), pk2(d[12], d[13]), pk2(d[14], d[15])};
            const unsigned o = OFF_KVP + (hp * 16 + m) * 3584 + ((head * 5 + 4 * ft + lh) * 21 + l31) * 16;
            st128c(c0, rws, (l31 < 21 && (ft == 0 || lh == 0)) ? o : OOB, 0);
            st128c(c1, rws, (l31 < 21 && ft == 0) ? o + 2 * 21 * 16 : OOB, 0);
        }
        wait_vm<0>();
        bar();
        STAMP(5);
        v4u wq[2];
        wq[0] = lds128(L_W1Q + l16);
#pragma unroll
        for (int i = 0; i < 40; ++i) {
            if (i + 1 < 40) wq[(i + 1) & 1] = lds128(L_W1Q + l16 + (i + 1) * 1024);
            acc[3] = mfma32(wq[i & 1], a[i], acc[3]);
        }
        bar();
        {
            const rsrc_t rx = mk_rsrc(x, SEQ * DM * 2);
            const unsigned o = ((64 * t + 16 * wv + l15) * 640 + 80 * c + 20 * lg) * 2;
            xr0 = ld128p(rx, o, 0); xr1 = ld128p(rx, o + 16, 0); xr2 = ld64p(rx, o + 32, 0);
        }
        float p[20];
#pragma unroll
        for (int i = 0; i < 20; ++i) p[i] = acc[(40 + i) >> 4][(40 + i) & 15];
        feature_map(p, phq);
        STAMP(6);
    } else {
        const int hw = wv - 4;
        const int i16 = (l31 & 3) + 4 * (l31 >> 3);
        const unsigned wl = lh * 2048 + ((l31 >> 2) & 1) * 1024 + i16 * 16;
        const unsigned wb = OFF_W1 + hp * 40 * 4096;
        auto issue = [&](int ch) {
            int s = 4 * ch + hw + rot1; s = s >= 40 ? s - 40 : s;
#pragma unroll
            for (int u = 0; u < 3; ++u) dma_c(rws, ((4 * ch + hw) * 3 + u) * 1024, wl, wb + s * 4096 + u * 256);
        };
        const int q = l15 >> 2, p = lane & 3;
        auto retile = [&](int slot) {
            int a0, a1; unsigned dst;
            if (slot < 5) {
                const int g = (slot * 4 + hw) * 4 + lg, gq = g < 70 ? g : 0, q10 = gq / 7, ti = gq - q10 * 7;
                const int r0 = q10 < 8 ? 20 * (q10 >> 1) + 8 * (q10 & 1) : 40 * (q10 - 8) + 16, r1 = q10 < 8 ? r0 + 4 : r0 + 20;
                const int cb = ti < 3 ? (12 * p + 4 * ti) * 2 : ti < 6 ? 9216 + (12 * p + 4 * (ti - 3)) * 2 : (p >> 1) * 9216 + (48 + 4 * (p & 1)) * 2;
                a0 = L_RAW + (r0 + q) * 112 + cb; a1 = L_RAW + (r1 + q) * 112 + cb;
                const int Q = 10 * TT + q10;
                dst = (g < 70 && j < 31) ? OFF_W3 + ((j * 20 + (Q >> 2)) * 7 + ti) * 1024 + ((Q & 3) * 16 + l15) * 16 : OOB;
                const v2u x0 = lds_tr(a0), x1 = lds_tr(a1);
                st128c(v4u{x0.x, x0.y, x1.x, x1.y}, rws, dst, 0);
                const unsigned one2 = 0x3f803f80u;
                const float cs = dot2(x0.x, one2, dot2(x0.y, one2, dot2(x1.x, one2, dot2(x1.y, one2, 0.f))));
                if (g < 70) *(LAS float*)(size_t)(unsigned)(L_CSC + (q10 * 112 + ti * 16 + l15) * 4) = cs;
            } else {
                if (slot < 8) {
                    const int g = ((slot - 5) * 4 + hw) * 4 + lg, gq = g < 35 ? g : 0, q7 = gq / 5, ti = gq - q7 * 5;
                    const int r0 = q7 < 4 ? 12 * q7 + q : q7 < 6 ? 24 * (q7 - 4) + 8 + q : 48 + 4 * (q >> 1) + (q & 1);
                    const int r1 = q7 < 4 ? r0 + 4 : q7 < 6 ? r0 + 12 : r0 + 2;
                    const int cb = 2 * 9216 + (20 * p + 4 * ti) * 2;
                    a0 = L_RAW + r0 * 160 + cb; a1 = L_RAW + r1 * 160 + cb;
                    const int Q = 7 * t + q7;
                    dst = (g < 35 && Q < 216) ? OFF_W4 + ((c * 54 + (Q >> 2)) * 5 + ti) * 1024 + ((Q & 3) * 16 + l15) * 16 : OOB;
                } else {
                    const int g = ((slot - 8) * 4 + hw) * 4 + lg, gq = g < 25 ? g : 0, q5 = gq / 5, ti = gq - q5 * 5;
                    const int r0 = q5 < 4 ? 20 * (q5 >> 1) + 8 * (q5 & 1) : 16, r1 = q5 < 4 ? r0 + 4 : 36;
                    const int cb = 3 * 9216 + (20 * p + 4 * ti) * 2;
                    a0 = L_RAW + (r0 + q) * 160 + cb; a1 = L_RAW + (r1 + q) * 160 + cb;
                    const int Q = 5 * t + q5;
                    dst = (g < 25 && t < 16) ? OFF_W2 + ((c * 20 + (Q >> 2)) * 5 + ti) * 1024 + ((Q & 3) * 16 + l15) * 16 : OOB;
                }
                const v2u x0 = lds_tr(a0), x1 = lds_tr(a1);
                st128c(v4u{x0.x, x0.y, x1.x, x1.y}, rws, dst, 0);
            }
        };
        issue(0); issue(1); issue(2);
        wait_vm<6>(); bar();
        issue(3); retile(0); wait_vm<7>(); bar();
        issue(4); retile(1); wait_vm<8>(); bar();
        issue(5); retile(2); wait_vm<9>(); bar();
        issue(6); retile(3); wait_vm<9>(); bar();
        issue(7); retile(4); wait_vm<9>(); bar();
        issue(8); retile(5); wait_vm<9>(); bar_lds();
        issue(9); retile(6); wait_vm<9>(); bar();
        retile(7); wait_vm<6>(); bar();
        retile(8); wait_vm<3>(); bar();
        retile(9);
        if (hw < 2) {
            const int n = hw * 64 + lane;
            float s = 0.f;
#pragma unroll
            for (int i = 0; i < 10; ++i) s += *(LAS float*)(size_t)(unsigned)(L_CSC + (i * 112 + (n < 112 ? n : 0)) * 4);
            st32c(__float_as_uint(s), rws, n < 112 ? OFF_CSP + (j * 8 + TT) * 512 + n * 4 : OOB, 0);
        }
        bar_lds();
        {
            const unsigned wl3 = lh * 2048 + ((l31 >> 2) & 1) * 1024 + (48 + (i16 < 12 ? i16 : 11)) * 16;
#pragma unroll
            for (int i = 0; i < 10; ++i) {
                int s = 4 * i + hw + rot1; s = s >= 40 ? s - 40 : s;
                dma_c(rws, L_W1Q + (4 * i + hw) * 1024, wl3, wb + s * 4096);
            }
        }
        wait_vm<10>();
        bar();
        if (wv == 7 && lane < 2) set_flag(ws, lane == 0 ? OFF_FWC + (c * 32 + t) * 8 : OFF_FWJ + (j * 16 + TT) * 8, tag);
        wait_vm<0>();
        bar();
        if (wv == 7 && lane == 0) set_flag(ws, OFF_FA + (hp * 16 + m) * 8, tag);
        bar();
        if (wv == 7) {
            if (lane < 5) lds_st128(L_ZR + lane * 336, v4u{0u, 0u, 0u, 0u});
            poll(ws, lane < 32 ? OFF_FWC + (c * 32 + lane) * 8 : lane < 48 ? OFF_FA + (hp * 16 + l15) * 8 : OFF_FWJ + (j * 16 + (lane & 7)) * 8, tag);
        }
    }
    bar();
    STAMP(7);

    if (wv < 7) {
        v4u g[8];
        const unsigned go = (32 * wv + l31) * 16 + lh * 8 * 3584;
#pragma unroll
        for (int i = 0; i < 8; ++i) g[i] = ld128c(rws, go, OFF_KVP + (hp * 16 + ((i + m) & 7)) * 3584);
        float v[8];
#pragma unroll
        for (int e = 0; e < 4; ++e) {
            float lo = 0.f, hi = 0.f;
#pragma unroll
            for (int i = 0; i < 8; ++i) { const unsigned u = g[i][e]; lo += bflo(u); hi += bfhi(u); }
            v[2 * e] = lo; v[2 * e + 1] = hi;
        }
#pragma unroll
        for (int e = 0; e < 4; ++e) { swap32f(v[e], v[e + 4]); v[e] += v[e + 4]; }
        lds_st64(L_KVT + (32 * wv + l31) * 16 + lh * 8, v2u{pk2(v[0], v[1]), pk2(v[2], v[3])});
    }
    bar_lds();
    STAMP(8);
    const int rot2 = (c * 5) >> 1, rot3 = ((j & 15) * 5) >> 2;
    unsigned hk[10];
    if (wv < 4) {
        {
            const int i16 = (l31 & 3) + 4 * (l31 >> 3);
            const bool mine = ((l31 >> 2) & 1) == lh;
            const int a0 = mine ? L_KVT + (lh * 105 + i16) * 16 : L_ZR, a1 = mine ? L_KVT + (lh * 105 + 16 + (i16 < 5 ? i16 : 4)) * 16 : L_ZR;
            f32x16 d0 = {}, d1 = {};
#pragma unroll
            for (int s = 0; s < 5; ++s) {
                const v4u bq = {phq[4 * s], phq[4 * s + 1], phq[4 * s + 2], phq[4 * s + 3]};
                d0 = mfma32(lds128(a0 + s * 336), bq, d0);
                d1 = mfma32(lds128(a1 + s * 336), bq, d1);
            }
            const float rd = __builtin_amdgcn_rcpf(d1[4] + 1e-6f);
            const v4u c0 = {pk2(d0[0] * rd, d0[1] * rd), pk2(d0[2] * rd, d0[3] * rd), pk2(d0[4] * rd, d0[5] * rd), pk2(d0[6] * rd, d0[7] * rd)};
            const v4u c1 = {pk2(d0[8] * rd, d0[9] * rd), pk2(d0[10] * rd, d0[11] * rd), pk2(d0[12] * rd, d0[13] * rd), pk2(d0[14] * rd, d0[15] * rd)};
            const v2u c2 = {pk2(d1[0] * rd, d1[1] * rd), pk2(d1[2] * rd, d1[3] * rd)};
            const unsigned o = OFF_O + (8 * m + 2 * wv + (l31 >> 4)) * 20480 + 5 * hp * 256 + l15 * 16;
            st128c(c0, rws, o + lh * 512, 0);
            st128c(c1, rws, o + lh * 512 + 256, 0);
            st64c(c2, rws, o + 1024 + lh * 8, 0);
        }
        wait_vm<0>();
        bar();
        STAMP(9);
        bar();
        STAMP(10);
        v4u a[20];
        const unsigned oo = OFF_O + (4 * t + wv) * 20480;
#pragma unroll
        for (int i = 0; i < 20; ++i) { int s = i + rot2; s = s >= 20 ? s - 20 : s; a[i] = ld128c(rws, l16, oo + s * 1024); }
        __builtin_amdgcn_sched_barrier(0);
        f32x4 acc[5] = {};
#pragma unroll
        for (int i = 0; i < 20; ++i)
#pragma unroll
            for (int u = 0; u < 5; ++u) acc[u] = mfma16(lds128(L_W2 + l16 + (i * 5 + u) * 1024), a[i], acc[u]);
        bar_lds();
        const unsigned xr[10] = {xr0.x, xr0.y, xr0.z, xr0.w, xr1.x, xr1.y, xr1.z, xr1.w, xr2.x, xr2.y};
        float s = 0.f, q = 0.f;
#pragma unroll
        for (int i = 0; i < 10; ++i) {
            hk[i] = pk2(acc[i >> 1][2 * (i & 1)] + bflo(xr[i]), acc[i >> 1][2 * (i & 1) + 1] + bfhi(xr[i]));
            s = dot2(hk[i], 0x3f803f80u, s); q = dot2(hk[i], hk[i], q);
        }
        const unsigned o = OFF_HB + (4 * t + wv) * 20480 + 10 * c * 256 + l15 * 16;
        st128c(v4u{hk[0], hk[1], hk[2], hk[3]}, rws, o + lg * 512, 0);
        st128c(v4u{hk[4], hk[5], hk[6], hk[7]}, rws, o + lg * 512 + 256, 0);
        st64c(v2u{hk[8], hk[9]}, rws, o + (8 + (lg >> 1)) * 256 + (lg & 1) * 8, 0);
        s = sum_x32(sum_x16(s)); q = sum_x32(sum_x16(q));
        st64c(v2u{__float_as_uint(s), __float_as_uint(q)}, rws, lane < 16 ? OFF_ST + ((4 * t + wv) * 8 + c) * 128 + lane * 8 : OOB, 0);
        wait_vm<0>();
        bar();
        STAMP(11);
        bar();
    } else {
        const int hw = wv - 4;
        auto w3dma = [&](int f) {
            const int i = f / 7, u = f - i * 7;
            int s = i + rot3; s = s >= 20 ? s - 20 : s;
            dma_c(rws, w3slot(i) + u * 1024, j < 31 ? l16 : OOB, OFF_W3 + ((j * 20 + s) * 7 + u) * 1024);
        };
        auto w2dma = [&](int f) {
            const int i = f / 5, u = f - i * 5;
            int s = i + rot2; s = s >= 20 ? s - 20 : s;
            dma_c(rws, L_W2 + f * 1024, l16, OFF_W2 + ((c * 20 + s) * 5 + u) * 1024);
        };
        if (hw < 3) {
#pragma unroll 1
            for (int f = hw; f < 60; f += 3) w2dma(f);
        } else {
            float s0 = 0.f, s1 = 0.f;
#pragma unroll
            for (int i = 0; i < 8; ++i) {
                s0 += __uint_as_float(ld32c(rws, lane * 4, OFF_CSP + (j * 8 + i) * 512));
                s1 += __uint_as_float(ld32c(rws, lane * 4 + 256, OFF_CSP + (j * 8 + i) * 512));
            }
            *(LAS float*)(size_t)(unsigned)(L_CS + lane * 4) = s0;
            if (lane < 48) *(LAS float*)(size_t)(unsigned)(L_CS + 256 + lane * 4) = s1;
        }
        bar();
        if (hw == 3) {
            if (lane == 0) set_flag(ws, OFF_FB + (m * 16 + hp) * 8, tag);
            poll(ws, OFF_FB + ((t >> 1) * 16 + l15) * 8, tag);
            if (b == 0 && lane == 0) set_flag(ws, OFF_EPOCH, tag);
        } else {
#pragma unroll 1
            for (int f = 60 + hw; f < 100; f += 3) w2dma(f);
#pragma unroll 1
            for (int f = hw; f < 49; f += 3) w3dma(f);
            wait_vm<16>();
        }
        bar_lds();
        bar();
        if (hw < 3) {
#pragma unroll 1
            for (int f = 49 + hw; f < 105; f += 3) w3dma(f);
        }
        bar();
        if (hw == 3) {
            if (lane == 0) set_flag(ws, OFF_FC + ((t >> 2) * 32 + (t & 3) * 8 + c) * 8, tag);
            poll(ws, OFF_FC + (TT * 32 + l31) * 8, tag);
        } else {
#pragma unroll 1
            for (int f = 105 + hw; f < 140; f += 3) w3dma(f);
            wait_vm<0>();
        }
        bar();
    }
    STAMP(12);

    const int grp = wv >> 2;
    if (j < 31) {
        const int tt0 = 16 * TT + 4 * (wv & 3) + 2 * grp;
        const unsigned ho = OFF_HB + tt0 * 20480;
        constexpr int PD = 8;
        v4u a[20][2];
        auto hld = [&](int i) { int s = i + rot3; s = s >= 20 ? s - 20 : s; a[i][0] = ld128c(rws, l16, ho + s * 1024); a[i][1] = ld128c(rws, l16, ho + 20480 + s * 1024); };
#pragma unroll
        for (int i = 0; i < PD; ++i) hld(i);
        v2u st[2][2];
#pragma unroll
        for (int k = 0; k < 2; ++k)
#pragma unroll
            for (int e = 0; e < 2; ++e) st[k][e] = ld64c(rws, (2 * lg + e) * 128 + l15 * 8, OFF_ST + (tt0 + k) * 1024);
        if (grp == 0) __builtin_amdgcn_s_setprio(1);
        f32x4 acc[2][7] = {};
        v4u w[2][7];
#pragma unroll
        for (int u = 0; u < 7; ++u) w[0][u] = lds128(w3slot(0) + l16 + u * 1024);
#pragma unroll
        for (int i = 0; i < 20; ++i) {
            if (i + PD < 20) hld(i + PD);
            if (i + 1 < 20) {
#pragma unroll
                for (int u = 0; u < 7; ++u) w[(i + 1) & 1][u] = lds128(w3slot(i + 1) + l16 + u * 1024);
            }
#pragma unroll
            for (int u = 0; u < 7; ++u) { acc[0][u] = mfma16(w[i & 1][u], a[i][0], acc[0][u]); acc[1][u] = mfma16(w[i & 1][u], a[i][1], acc[1][u]); }
            __builtin_amdgcn_sched_barrier(0);
        }
        if (grp == 0) __builtin_amdgcn_s_setprio(0);
        STAMP(13);
        f32x4 cs[7];
#pragma unroll
        for (int u = 0; u < 7; ++u) cs[u] = *(LAS f32x4*)(size_t)(unsigned)(L_CS + (u * 16 + lg * 4) * 4);
        if (grp == 1) bar_lds();
        unsigned hh[2][7];
#pragma unroll
        for (int k = 0; k < 2; ++k) {
            const float s = sum_x32(sum_x16(__uint_as_float(st[k][0].x) + __uint_as_float(st[k][1].x)));
            const float q = sum_x32(sum_x16(__uint_as_float(st[k][0].y) + __uint_as_float(st[k][1].y)));
            const float mean = s * (1.f / 640.f), var = fmaxf(q * (1.f / 640.f) - mean * mean, 0.f);
            const float rs = __builtin_amdgcn_rsqf(var + 1e-5f), nb = -mean * rs;
            float z[7][4];
#pragma unroll
            for (int u = 0; u < 7; ++u)
#pragma unroll
                for (int r = 0; r < 4; ++r) z[u][r] = __builtin_fmaf(acc[k][u][r], rs, nb * cs[u][r]);
#pragma unroll
            for (int u = 0; u < 3; ++u) {
                hh[k][2 * u] = pk2(geglu(z[u][0], z[u + 3][0]), geglu(z[u][1], z[u + 3][1]));
                hh[k][2 * u + 1] = pk2(geglu(z[u][2], z[u + 3][2]), geglu(z[u][3], z[u + 3][3]));
            }
            swap32f(z[6][0], z[6][2]); swap32f(z[6][1], z[6][3]);
            hh[k][6] = pk2(geglu(z[6][0], z[6][2]), geglu(z[6][1], z[6][3]));
        }
        const unsigned o = OFF_HH + tt0 * 55296 + 7 * j * 256 + l15 * 16;
        st128c(v4u{hh[0][0], hh[0][1], hh[0][2], hh[0][3]}, rws, o + lg * 256, 0);
        st128c(v4u{hh[1][0], hh[1][1], hh[1][2], hh[1][3]}, rws, o + lg * 256, 55296);
        swap16(hh[0][4], hh[1][4]); swap16(hh[0][5], hh[1][5]); swap16(hh[0][6], hh[1][6]);
        st128c(v4u{hh[0][4], hh[0][5], hh[1][4], hh[1][5]}, rws, o + (lg & 1) * 55296 + (4 + (lg >> 1)) * 256, 0);
        st64c(v2u{hh[0][6], hh[1][6]}, rws, j < 30 ? o + (lg & 1) * 55296 + 6 * 256 + (lg >> 1) * 8 : OOB, 0);
        wait_vm<0>();
        if (lane == 0) set_flag(ws, OFF_FD + (((4 * TT + (wv & 3)) * 2 + grp) * 32 + j) * 8, tag);
        STAMP(14);
    } else if (grp == 1) {
        bar();
    }

    if (grp == 0) {
        const int k0 = wv < 2 ? 14 * wv : 28 + 13 * (wv - 2), nk = wv < 2 ? 14 : 13, rot4 = (c * 13) >> 3;
        auto ks = [&](int i) { int s = i + rot4; return k0 + (s >= nk ? s - nk : s); };
        const unsigned wb = OFF_W4 + c * 270 * 1024, hb = OFF_HH + 4 * t * 55296;
        v4u wr[3][5];
#pragma unroll
        for (int i = 0; i < 3; ++i)
#pragma unroll
            for (int u = 0; u < 5; ++u) wr[i][u] = ld128c(rws, l16 + u * 1024, wb + ks(i) * 5120);
        if (wv == 0) poll(ws, OFF_FD + ((t * 2 + 0) * 32 + (l31 < 31 ? l31 : 30)) * 8, tag);
        bar();
        STAMP(15);
        const int lw = wv * 40960;
#pragma unroll
        for (int i = 3; i < 11; ++i)
#pragma unroll
            for (int u = 0; u < 5; ++u) dma_c(rws, lw + ((i - 3) * 5 + u) * 1024, l16, wb + ks(i) * 5120 + u * 1024);
        wait_vm<0>();
        f32x4 acc[4][5] = {};
        auto pass = [&](int k) {
            v4u a[11][2];
            constexpr int PD = 6;
            auto hld = [&](int i) { a[i][0] = ld128c(rws, l16, hb + k * 55296 + ks(i) * 1024); a[i][1] = ld128c(rws, l16, hb + (k + 1) * 55296 + ks(i) * 1024); };
#pragma unroll
            for (int i = 0; i < PD; ++i) hld(i);
#pragma unroll
            for (int i = 0; i < 11; ++i) {
                if (i + PD < 11) hld(i + PD);
#pragma unroll
                for (int u = 0; u < 5; ++u) {
                    const v4u w = i < 3 ? wr[i < 3 ? i : 0][u] : lds128(lw + l16 + ((i - 3) * 5 + u) * 1024);
                    acc[k][u] = mfma16(w, a[i][0], acc[k][u]); acc[k + 1][u] = mfma16(w, a[i][1], acc[k + 1][u]);
                }
            }
        };
        pass(0);
        STAMP(16);
        bar();
        STAMP(17);
        pass(2);
        {
            v4u a[3][4];
#pragma unroll
            for (int i = 0; i < 3; ++i) {
                const bool on = i < 2 || nk == 14;
#pragma unroll
                for (int u = 0; u < 5; ++u) wr[i][u] = ld128c(rws, on ? l16 + u * 1024 : OOB, wb + ks(11 + i) * 5120);
#pragma unroll
                for (int k = 0; k < 4; ++k) a[i][k] = ld128c(rws, on ? l16 : OOB, hb + k * 55296 + ks(11 + i) * 1024);
            }
#pragma unroll
            for (int i = 0; i < 3; ++i)
#pragma unroll
                for (int u = 0; u < 5; ++u)
#pragma unroll
                    for (int k = 0; k < 4; ++k) acc[k][u] = mfma16(wr[i][u], a[i][k], acc[k][u]);
        }
        STAMP(18);
#pragma unroll
        for (int k = 0; k < 4; ++k)
#pragma unroll
            for (int u = 0; u < 5; ++u) *(LAS f32x4*)(size_t)(unsigned)(lw + (k * 5 + u) * 1024 + l16) = acc[k][u];
        bar_lds();
        {
            f32x4 r[5];
#pragma unroll
            for (int u = 0; u < 5; ++u) {
                r[u] = *(LAS f32x4*)(size_t)(unsigned)((wv * 5 + u) * 1024 + l16);
#pragma unroll
                for (int k = 1; k < 4; ++k) r[u] += *(LAS f32x4*)(size_t)(unsigned)(k * 40960 + (wv * 5 + u) * 1024 + l16);
            }
            unsigned y[10];
#pragma unroll
            for (int i = 0; i < 10; ++i) y[i] = pk2(r[i >> 1][2 * (i & 1)] + bflo(hk[i]), r[i >> 1][2 * (i & 1) + 1] + bfhi(hk[i]));
            const rsrc_t ro = mk_rsrc(out, SEQ * DM * 2);
            const unsigned o = ((64 * t + 16 * wv + l15) * 640 + 80 * c + 20 * lg) * 2;
            __builtin_amdgcn_raw_buffer_store_b128(v4u{y[0], y[1], y[2], y[3]}, ro, o, 0, 0);
            __builtin_amdgcn_raw_buffer_store_b128(v4u{y[4], y[5], y[6], y[7]}, ro, o + 16, 0, 0);
            __builtin_amdgcn_raw_buffer_store_b64(v2u{y[8], y[9]}, ro, o + 32, 0, 0);
        }
        STAMP(19);
        asm volatile("fb_code_hi:");
    } else {
        if (wv == 7) poll(ws, OFF_FD + ((t * 2 + 1) * 32 + (l31 < 31 ? l31 : 30)) * 8, tag);
        bar();
        bar();
    }
}

}

extern "C" void kernel_launch(void* const* d_in, const int* in_sizes, int n_in, void* d_out, int out_size, void* d_ws, size_t ws_size,
                              hipStream_t stream) {
    (void)in_sizes; (void)n_in; (void)out_size; (void)ws_size;
    fused_block<<<256, 512, 0, stream>>>((const u16*)d_in[0], (const u16*)d_in[1], (const u16*)d_in[2], (const u16*)d_in[3],
                                                 (const u16*)d_in[5], (const u16*)d_in[6], (const u16*)d_in[7], (u16*)d_out, (char*)d_ws);
}
```

```cpp
#define AMP_A 0
#define AMP_B 0
#include <hip/hip_runtime.h>
#include <stdint.h>

typedef unsigned short u16;
typedef unsigned long long u64;
typedef unsigned v4u __attribute__((ext_vector_type(4)));
typedef unsigned v2u __attribute__((ext_vector_type(2)));
typedef float f32x2 __attribute__((ext_vector_type(2)));
typedef float f32x4 __attribute__((ext_vector_type(4)));
typedef float f32x16 __attribute__((ext_vector_type(16)));
typedef __bf16 bf16x2 __attribute__((ext_vector_type(2)));
typedef __bf16 bf16x8 __attribute__((ext_vector_type(8)));
typedef short s16x4 __attribute__((ext_vector_type(4)));
typedef __amdgpu_buffer_rsrc_t rsrc_t;
#define GAS __attribute__((address_space(1)))
#define LAS __attribute__((address_space(3)))
#define DI __device__ __forceinline__

namespace {

constexpr int SEQ = 2048, DM = 640, FF = 1728;

constexpr unsigned OFF_EPOCH = 0;
constexpr unsigned OFF_F0  = 4096;
constexpr unsigned OFF_FA  = OFF_F0 + 32768;
constexpr unsigned OFF_FB  = OFF_FA + 32768;
constexpr unsigned OFF_FC  = OFF_FB + 32768;
constexpr unsigned OFF_FWC = OFF_FC + 32768;
constexpr unsigned OFF_FWJ = OFF_FWC + 32768;
constexpr unsigned OFF_FW4 = OFF_FWJ + 32768;
constexpr unsigned OFF_FD  = OFF_FW4 + 32768;
constexpr unsigned OFF_W1  = OFF_FD + 262144;
constexpr unsigned OFF_C1P = OFF_W1 + 16 * 163840;
constexpr unsigned OFF_KVP = OFF_C1P + 256 * 640;
constexpr unsigned OFF_O   = OFF_KVP + 256 * 3584;
constexpr unsigned OFF_W2  = OFF_O + 128 * 20480;
constexpr unsigned OFF_HB  = OFF_W2 + 8 * 100 * 1024;
constexpr unsigned OFF_ST  = OFF_HB + 128 * 20480;
constexpr unsigned OFF_CSP = OFF_ST + 128 * 8 * 128;
constexpr unsigned OFF_W3  = OFF_CSP + 32 * 8 * 512;
constexpr unsigned OFF_HH  = OFF_W3 + 31 * 140 * 1024;
constexpr unsigned OFF_W4  = OFF_HH + 128 * 216 * 256;
constexpr unsigned WS_USED = OFF_W4 + 8 * 270 * 1024;
constexpr unsigned OFF_STAMP = (WS_USED + 4095) & ~4095u;
constexpr unsigned OOB = 0x7ffffff0u;

constexpr int LDS_BYTES = 163840;
constexpr int L_X    = 0;
constexpr int L_F    = 131072;
constexpr int L_PF   = 152576;
constexpr int L_RG = 0, L_RU = L_RG + 9216, L_RD = L_RU + 9216, L_RO = L_RD + 9600;
constexpr int L_W1Q  = 122880;
constexpr int L_PKV  = 40960;
constexpr int L_KVT  = 153600;
constexpr int L_ZR   = L_KVT + 3584;
constexpr int L_CS   = L_ZR + 1376;
constexpr int L_C1   = L_CS + 512;
constexpr int L_CNT  = 163584;
constexpr int L_PRG = L_CNT + 16, L_W3R = L_CNT + 32, L_W2D = L_CNT + 48, L_TG = L_CNT + 64;
__host__ __device__ constexpr int w2frag(int f) { return f < 40 ? f * 1024 : f < 84 ? 77824 + (f - 40) * 1024 : f < 99 ? 62464 + (f - 84) * 1024 : 151552; }
__host__ __device__ constexpr int w3slot(int i) { return i < 3 ? 40960 + i * 7168 : i < 7 ? 122880 + (i - 3) * 7168 : i < 12 ? (i - 7) * 7168 : 62464 + (i - 12) * 7168; }

#define CODEPF 0
#define RAW_SLEEP 0
#define W4_SLEEP 12
#define P0_DELAY 12
#define PA_DELAY 14
#define PB_DELAY 14
#define PC_DELAY 14
#define W4_N1 0
#define W4_N2 10
#define D_SLEEP 14
#define RT_SLEEP 6
#define W3_PACE 1
#define W4_PACE 2
#define P3_FLAG_AT 4
#define P3_LPS 4
#define P4_PD1 6
#define P4_PD2 6
#define MAPY 1
#define ROT 0
#define P0_PRIO 3
#define XRING 6
#define A_SLEEP 12
#define B_SLEEP 0
#define C_SLEEP 4
#define P3_PDB 18
#define P3_PACE 4
#define P1_FLAG_AT 20
#define P2_PD 12
#define P2_SLEEP 24
#define P3_PD 7
#define P3_SLEEP 40
#define POLL_SLEEP 1
constexpr int AUX_SC1 = 16;
constexpr float LOG2E = 1.4426950408889634f;

DI rsrc_t mk_rsrc(const void* p, unsigned bytes) { return __builtin_amdgcn_make_buffer_rsrc((void*)p, 0, bytes, 0x00020000); }
DI v4u ld128c(rsrc_t r, unsigned v, unsigned s) { return __builtin_amdgcn_raw_buffer_load_b128(r, v, s, AUX_SC1); }
DI v2u ld64c(rsrc_t r, unsigned v, unsigned s) { return __builtin_amdgcn_raw_buffer_load_b64(r, v, s, AUX_SC1); }
DI unsigned ld32c(rsrc_t r, unsigned v, unsigned s) { return __builtin_amdgcn_raw_buffer_load_b32(r, v, s, AUX_SC1); }
DI v4u ld128p(rsrc_t r, unsigned v, unsigned s) { return __builtin_amdgcn_raw_buffer_load_b128(r, v, s, 0); }
DI v2u ld64p(rsrc_t r, unsigned v, unsigned s) { return __builtin_amdgcn_raw_buffer_load_b64(r, v, s, 0); }
DI void st128c(v4u d, rsrc_t r, unsigned v, unsigned s) { __builtin_amdgcn_raw_buffer_store_b128(d, r, v + s, 0, AUX_SC1); }
DI void st64c(v2u d, rsrc_t r, unsigned v, unsigned s) { __builtin_amdgcn_raw_buffer_store_b64(d, r, v, s, AUX_SC1); }
DI void st32c(unsigned d, rsrc_t r, unsigned v, unsigned s) { __builtin_amdgcn_raw_buffer_store_b32(d, r, v, s, AUX_SC1); }

DI void dma_c(rsrc_t r, unsigned lds, unsigned v, unsigned s) {
    asm volatile("s_mov_b32 m0, %0\n\ts_nop 0\n\tbuffer_load_dwordx4 %1, %2, %3 offen sc1 lds" ::"s"(lds), "v"(v), "s"(r), "s"(s) : "memory");
}
DI void dma_p(rsrc_t r, unsigned lds, unsigned v, unsigned s) {
    asm volatile("s_mov_b32 m0, %0\n\ts_nop 0\n\tbuffer_load_dwordx4 %1, %2, %3 offen lds" ::"s"(lds), "v"(v), "s"(r), "s"(s) : "memory");
}
template <int N> DI void dma_run(rsrc_t r, unsigned lds, unsigned v, unsigned s) {
    if constexpr (N >= 4) {
        asm volatile("s_mov_b32 m0, %0\n\ts_nop 0\n\tbuffer_load_dwordx4 %1, %2, %3 offen sc1 lds\n\tbuffer_load_dwordx4 %1, %2, %3 offen offset:1024 sc1 lds\n\t"
                     "buffer_load_dwordx4 %1, %2, %3 offen offset:2048 sc1 lds\n\tbuffer_load_dwordx4 %1, %2, %3 offen offset:3072 sc1 lds" ::"s"(lds), "v"(v), "s"(r), "s"(s) : "memory");
        dma_run<N - 4>(r, lds + 4096, v, s + 4096);
    } else if constexpr (N > 0) {
        dma_c(r, lds, v, s);
        dma_run<N - 1>(r, lds + 1024, v, s + 1024);
    }
}
DI void warm(rsrc_t r, unsigned lds, unsigned v, unsigned s) {
    asm volatile("s_mov_b32 m0, %0\n\ts_nop 0\n\tbuffer_load_dword %1, %2, %3 offen sc1 lds" ::"s"(lds), "v"(v), "s"(r), "s"(s) : "memory");
}
template <int N> DI void wait_vm() { asm volatile("s_waitcnt vmcnt(%0)" ::"n"(N) : "memory"); }
DI void wait_lds() { asm volatile("s_waitcnt lgkmcnt(0)" ::: "memory"); }
DI void bar() { asm volatile("s_barrier" ::: "memory"); }
DI void bar_lds() { asm volatile("s_waitcnt lgkmcnt(0)\n\ts_barrier" ::: "memory"); }
template <int N, int EXTRA> DI void drain_steps() { wait_vm<6 * N + EXTRA>(); bar(); if constexpr (N > 0) drain_steps<N - 1, EXTRA>(); }

template <int N> struct ic { static constexpr int value = N; };
template <class T> DI LAS T* lp(int off) { return (LAS T*)(size_t)(unsigned)off; }
DI v4u lds128(int off) { return *lp<v4u>(off); }
DI void lds_st128(int off, v4u d) { *lp<v4u>(off) = d; }
DI void lds_st64(int off, v2u d) { *lp<v2u>(off) = d; }
DI v2u lds_tr(int off) { return __builtin_bit_cast(v2u, __builtin_amdgcn_ds_read_tr16_b64_v4i16(lp<s16x4>(off))); }
DI v4u lds_tr2(int a0, int a1) { const v2u x0 = lds_tr(a0), x1 = lds_tr(a1); return v4u{x0.x, x0.y, x1.x, x1.y}; }

DI f32x16 mfma32(v4u a, v4u b, f32x16 c) { return __builtin_amdgcn_mfma_f32_32x32x16_bf16(__builtin_bit_cast(bf16x8, a), __builtin_bit_cast(bf16x8, b), c, 0, 0, 0); }
DI f32x4 mfma16(v4u a, v4u b, f32x4 c) { return __builtin_amdgcn_mfma_f32_16x16x32_bf16(__builtin_bit_cast(bf16x8, a), __builtin_bit_cast(bf16x8, b), c, 0, 0, 0); }
template <bool FIRST> DI void mma16(f32x4& c, v4u a, v4u b) {
    if constexpr (FIRST) asm volatile("v_mfma_f32_16x16x32_bf16 %0, %1, %2, 0" : "=&v"(c) : "v"(a), "v"(b) : "memory");
    else asm volatile("v_mfma_f32_16x16x32_bf16 %0, %1, %2, %0" : "+v"(c) : "v"(a), "v"(b) : "memory");
}
template <bool FIRST> DI void mma32(f32x16& c, v4u a, v4u b) {
    if constexpr (FIRST) asm volatile("v_mfma_f32_32x32x16_bf16 %0, %1, %2, 0" : "=&v"(c) : "v"(a), "v"(b) : "memory");
    else asm volatile("v_mfma_f32_32x32x16_bf16 %0, %1, %2, %0" : "+v"(c) : "v"(a), "v"(b) : "memory");
}
DI void mma16f(bool first, f32x4& c, v4u a, v4u b) { if (first) mma16<true>(c, a, b); else mma16<false>(c, a, b); }
DI void mma32f(bool first, f32x16& c, v4u a, v4u b) { if (first) mma32<true>(c, a, b); else mma32<false>(c, a, b); }
DI void mma_end(f32x16& a) { asm volatile("s_nop 7\n\ts_nop 4" : "+v"(a)); }
DI void mma_end(f32x16& a, f32x16& b, f32x16& c) { asm volatile("s_nop 7\n\ts_nop 4" : "+v"(a), "+v"(b), "+v"(c)); }
DI void mma_end(f32x4 (&a)[5]) { asm volatile("s_nop 7\n\ts_nop 0" : "+v"(a[0]), "+v"(a[1]), "+v"(a[2]), "+v"(a[3]), "+v"(a[4])); }
DI void mma_end(f32x4 (&a)[7]) { asm volatile("s_nop 7\n\ts_nop 0" : "+v"(a[0]), "+v"(a[1]), "+v"(a[2]), "+v"(a[3]), "+v"(a[4]), "+v"(a[5]), "+v"(a[6])); }
struct Frag {
    unsigned b0, b1, b2;
    DI explicit Frag(unsigned l16) : b0(l16), b1(l16 + 65536), b2(l16 + 131072) { asm volatile("" : "+v"(b0), "+v"(b1), "+v"(b2)); }
    DI v4u operator()(int off) const { return off < 65536 ? lds128(b0 + off) : off < 131072 ? lds128(b1 + (off - 65536)) : lds128(b2 + (off - 131072)); }
};
DI unsigned pk2v(f32x2 p) { bf16x2 q = __builtin_convertvector(p, bf16x2); return __builtin_bit_cast(unsigned, q); }
DI unsigned pk2(float lo, float hi) { return pk2v(f32x2{lo, hi}); }
DI float bflo(unsigned u) { return __uint_as_float(u << 16); }
DI float bfhi(unsigned u) { return __uint_as_float(u & 0xffff0000u); }
DI float dot2(unsigned a, unsigned b, float c) { return __builtin_amdgcn_fdot2_f32_bf16(__builtin_bit_cast(bf16x2, a), __builtin_bit_cast(bf16x2, b), c, false); }
DI float sum8(v4u v, float a) { unsigned x0 = v.x, x1 = v.y, x2 = v.z, x3 = v.w; return dot2(x0, 0x3f803f80u, dot2(x1, 0x3f803f80u, dot2(x2, 0x3f803f80u, dot2(x3, 0x3f803f80u, a)))); }
DI float sum_x16(float v) { unsigned u = __float_as_uint(v); auto r = __builtin_amdgcn_permlane16_swap(u, u, false, false); unsigned a = r[0], b = r[1]; return __uint_as_float(a) + __uint_as_float(b); }
DI float sum_x32(float v) { unsigned u = __float_as_uint(v); auto r = __builtin_amdgcn_permlane32_swap(u, u, false, false); unsigned a = r[0], b = r[1]; return __uint_as_float(a) + __uint_as_float(b); }
DI void swap16(unsigned& a, unsigned& b) { auto r = __builtin_amdgcn_permlane16_swap(a, b, false, false); a = r[0]; b = r[1]; }
DI void swap32(unsigned& a, unsigned& b) { auto r = __builtin_amdgcn_permlane32_swap(a, b, false, false); a = r[0]; b = r[1]; }
DI void swap32f(float& a, float& b) { unsigned x = __float_as_uint(a), y = __float_as_uint(b); swap32(x, y); a = __uint_as_float(x); b = __uint_as_float(y); }
DI f32x2 exp2x2(f32x2 v) { return f32x2{__builtin_amdgcn_exp2f(v.x), __builtin_amdgcn_exp2f(v.y)}; }
DI f32x2 fma2(f32x2 a, f32x2 b, f32x2 c) { return __builtin_elementwise_fma(a, b, c); }
DI f32x2 sp2(float v) { return f32x2{v, v}; }

DI void set_flag(char* ws, unsigned off, u64 tag) { __hip_atomic_store((GAS u64*)(ws + off), tag, __ATOMIC_RELAXED, __HIP_MEMORY_SCOPE_AGENT); }
DI void arrive4(int cnt, int lane, char* ws, unsigned off, u64 tag) {
    if (lane == 0) {
        const unsigned old = __hip_atomic_fetch_add(lp<unsigned>(cnt), 1u, __ATOMIC_RELAXED, __HIP_MEMORY_SCOPE_WORKGROUP);
        if ((old & 3u) == 3u) set_flag(ws, off, tag);
    }
}
DI void lds_wait(int off, u64 tag) {
    for (int sp = 0; sp < (1 << 22); ++sp)
        if (__all(*lp<volatile u64>(off) == tag)) break; else __builtin_amdgcn_s_sleep(1);
    asm volatile("" ::: "memory");
}
DI void poll(char* ws, unsigned off, u64 tag) {
    GAS u64* p = (GAS u64*)(ws + off);
    for (int sp = 0; sp < (1 << 17); ++sp) {
        if (__hip_atomic_load(p, __ATOMIC_RELAXED, __HIP_MEMORY_SCOPE_AGENT) == tag) break;
        if (POLL_SLEEP) __builtin_amdgcn_s_sleep(POLL_SLEEP);
    }
    __builtin_amdgcn_fence(__ATOMIC_ACQUIRE, "wavefront");
    asm volatile("" ::: "memory");
}
DI void lds_wait_ge(int off, unsigned n) {
    for (int sp = 0; sp < (1 << 22); ++sp)
        if (__all(*lp<volatile unsigned>(off) >= n)) break; else __builtin_amdgcn_s_sleep(1);
    asm volatile("" ::: "memory");
}

DI void feature_map(const f32x2 (&z)[10], float rs, unsigned (&o)[20]) {
    float mx = fmaxf(z[0].x, z[0].y), mn = fminf(z[0].x, z[0].y);
#pragma unroll
    for (int i = 1; i < 10; ++i) { mx = fmaxf(fmaxf(mx, z[i].x), z[i].y); mn = fminf(fminf(mn, z[i].x), z[i].y); }
    f32x2 ep[10], en[10];
#pragma unroll
    for (int i = 0; i < 10; ++i) { ep[i] = exp2x2(fma2(z[i], sp2(rs), sp2(-rs * mx))); en[i] = exp2x2(fma2(z[i], sp2(-rs), sp2(rs * mn))); }
    f32x2 sp = ep[0], sn = en[0];
#pragma unroll
    for (int i = 1; i < 10; ++i) { sp += ep[i]; sn += en[i]; }
    const float rp = __builtin_amdgcn_rcpf(sp.x + sp.y), rn = __builtin_amdgcn_rcpf(sn.x + sn.y);
#pragma unroll
    for (int i = 0; i < 10; ++i) { o[i] = pk2v(ep[i] * rp); o[10 + i] = pk2v(en[i] * rn); }
}

DI f32x2 geglu2(f32x2 G, f32x2 U, float K1, float K2, float IV) {
    const f32x2 e = exp2x2(G * fma2(G * G, sp2(K2), sp2(K1)));
    const f32x2 d = fma2(e, sp2(IV), sp2(IV));
    return G * U * f32x2{__builtin_amdgcn_rcpf(d.x), __builtin_amdgcn_rcpf(d.y)};
}
DI void p3_epilogue(const f32x4 (&acc)[2][7], const f32x4 (&cs)[7], const v2u (&st)[2][2], unsigned (&hh)[2][7]) {
    float c6[4] = {cs[6].x, cs[6].y, cs[6].z, cs[6].w};
    swap32f(c6[0], c6[2]); swap32f(c6[1], c6[3]);
#pragma unroll
    for (int k = 0; k < 2; ++k) {
        const float s = sum_x32(sum_x16(__uint_as_float(st[k][0].x) + __uint_as_float(st[k][1].x)));
        const float q = sum_x32(sum_x16(__uint_as_float(st[k][0].y) + __uint_as_float(st[k][1].y)));
        const float nm = s * (-1.f / 640.f), IV = fmaxf(q * (1.f / 640.f) - nm * nm, 0.f) + 1e-5f;
        const float rs = __builtin_amdgcn_rsqf(IV);
        const float K1 = (-2.f * 0.7978845608028654f * LOG2E) * rs, K2 = (0.044715f * K1) * (rs * rs);
        float z6[4] = {acc[k][6].x, acc[k][6].y, acc[k][6].z, acc[k][6].w};
        swap32f(z6[0], z6[2]); swap32f(z6[1], z6[3]);
#pragma unroll
        for (int u = 0; u < 3; ++u) {
            hh[k][2 * u] = pk2v(geglu2(fma2(cs[u].xy, sp2(nm), acc[k][u].xy), fma2(cs[u + 3].xy, sp2(nm), acc[k][u + 3].xy), K1, K2, IV));
            hh[k][2 * u + 1] = pk2v(geglu2(fma2(cs[u].zw, sp2(nm), acc[k][u].zw), fma2(cs[u + 3].zw, sp2(nm), acc[k][u + 3].zw), K1, K2, IV));
        }
        hh[k][6] = pk2v(geglu2(fma2(f32x2{c6[0], c6[1]}, sp2(nm), f32x2{z6[0], z6[1]}), fma2(f32x2{c6[2], c6[3]}, sp2(nm), f32x2{z6[2], z6[3]}), K1, K2, IV));
    }
}

#define STAMP(i) do { if ((i) == AMP_A) ampa = __builtin_amdgcn_s_memrealtime(); if ((i) == AMP_B) ampb = __builtin_amdgcn_s_memrealtime(); } while (0)
#define CSTAMP(i) do {} while (0)
#define SB() __builtin_amdgcn_sched_barrier(0)

__global__ __launch_bounds__(512, 2) void fused_block(const u16* __restrict__ x, const u16* __restrict__ wqkv, const u16* __restrict__ wphi,
                                                      const u16* __restrict__ wo, const u16* __restrict__ wg, const u16* __restrict__ wu,
                                                      const u16* __restrict__ wd, u16* __restrict__ out, char* __restrict__ ws) {
    __shared__ __attribute__((aligned(16))) char smem[LDS_BYTES];
    asm volatile("" ::"v"((LAS char*)smem));
    u64 ampa = 0, ampb = 0;
    STAMP(0); CSTAMP(62);
    asm volatile("" ::"s"(x), "s"(wqkv), "s"(wphi), "s"(wo), "s"(wg), "s"(wu), "s"(wd), "s"(out), "s"(ws));
    const int tid = threadIdx.x, lane = tid & 63;
    const int wv = __builtin_amdgcn_readfirstlane(tid >> 6);
    const int b = blockIdx.x, xcd = b & 7, rk = b >> 3;
    u64 pc; unsigned clen;
    asm volatile("s_getpc_b64 %0\nfb_code_lo:\n\ts_mov_b32 %1, fb_code_hi-fb_code_lo" : "=s"(pc), "=s"(clen));
    auto codepf = [&]() {
        const rsrc_t rc = mk_rsrc((const void*)pc, clen);
        const unsigned v = __builtin_amdgcn_raw_buffer_load_b32(rc, lane < 20 ? (lane * 32 + rk) * 128 : OOB, 0, 0);
        asm volatile("" ::"v"(v));
    };
    if (wv == 3) {
        const rsrc_t rc = mk_rsrc((const void*)pc, clen);
        if (lane < 20) asm volatile("s_mov_b32 m0, %0\n\ts_nop 0\n\tbuffer_load_dword %1, %2, 0 offen lds" ::"s"(L_PF), "v"((lane * 32 + rk) * 128), "s"(rc) : "memory");
        if (lane < 16) *lp<unsigned>(L_CNT + lane * 4) = 0u;
    }
    const int m = (xcd >> 1) * 4 + (rk >> 3), hp = (xcd & 1) * 8 + (rk & 7);
    const int t = (xcd >> 1) * 8 + (rk >> 2), c = (xcd & 1) * 4 + (rk & 3);
    const int TT = (xcd >> 2) * 4 + (rk >> 3), j = (xcd & 3) * 8 + (rk & 7);
    const rsrc_t rws = mk_rsrc(ws, WS_USED);
    const int l15 = lane & 15, lg = lane >> 4, l31 = lane & 31, lh = lane >> 5;
    const unsigned l16 = lane * 16;
    const v2u ep = ld64c(rws, OFF_EPOCH, 0);
    u64 tag;
    auto gather = [&]() {
        v4u g[8];
        const unsigned go = (32 * wv + l31) * 16 + lh * 8 * 3584;
#pragma unroll
        for (int i = 0; i < 8; ++i) g[i] = ld128c(rws, go, OFF_KVP + (hp * 16 + ((i + 2 * m) & 7)) * 3584);
        SB();
        f32x2 v[4] = {};
#pragma unroll
        for (int i = 0; i < 8; ++i)
#pragma unroll
            for (int e = 0; e < 4; ++e) { const unsigned u = g[i][e]; v[e] += f32x2{bflo(u), bfhi(u)}; }
        float z[8] = {v[0].x, v[0].y, v[1].x, v[1].y, v[2].x, v[2].y, v[3].x, v[3].y};
#pragma unroll
        for (int e = 0; e < 4; ++e) { swap32f(z[e], z[e + 4]); z[e] += z[e + 4]; }
        lds_st64(L_KVT + (32 * wv + l31) * 16 + lh * 8, v2u{pk2(z[0], z[1]), pk2(z[2], z[3])});
    };
    if (wv < 4) {
        unsigned phq[20];
        v4u xr0, xr1; v2u xr2;
        {
        v4u a[40];
        float mean1, rs1;
        {
            const rsrc_t rx = mk_rsrc(x, SEQ * DM * 2);
            const int lx = L_X + wv * 32768;
            unsigned vo[2];
#pragma unroll
            for (int g = 0; g < 2; ++g) vo[g] = (lane >> 3) * 1280 + (((lane & 7) ^ ((4 * g + (lane >> 4)) & 7)) * 16);
            const unsigned so = (128 * m + 32 * wv) * 1280;
            auto blk = [&](int bb) {
#pragma unroll
                for (int g = 0; g < 4; ++g) dma_p(rx, lx + (bb % 8) * 4096 + g * 1024, vo[g & 1], so + g * 10240 + bb * 128);
            };
#pragma unroll
            for (int bb = 0; bb < 8; ++bb) blk(bb);
            STAMP(20);
            unsigned ra[4];
#pragma unroll
            for (int s = 0; s < 4; ++s) ra[s] = lx + l31 * 128 + (((2 * s + lh) ^ ((l31 >> 1) & 7)) * 16);
            float s0 = 0.f, s1 = 0.f, q0 = 0.f, q1 = 0.f;
            auto rd = [&](int bb) {
#pragma unroll
                for (int s = 0; s < 4; ++s) a[4 * bb + s] = lds128(ra[s] + (bb % 8) * 4096);
            };
            auto stat = [&](int bb) {
#pragma unroll
                for (int s = 0; s < 4; ++s) {
                    const unsigned e0 = a[4 * bb + s].x, e1 = a[4 * bb + s].y, e2 = a[4 * bb + s].z, e3 = a[4 * bb + s].w;
                    s0 = dot2(e0, 0x3f803f80u, s0); q0 = dot2(e0, e0, q0); s1 = dot2(e1, 0x3f803f80u, s1); q1 = dot2(e1, e1, q1);
                    s0 = dot2(e2, 0x3f803f80u, s0); q0 = dot2(e2, e2, q0); s1 = dot2(e3, 0x3f803f80u, s1); q1 = dot2(e3, e3, q1);
                }
                SB();
            };
            wait_vm<24>(); STAMP(21); rd(0); rd(1); wait_lds(); blk(8); blk(9);
            STAMP(45);
            wait_vm<24>(); rd(2); rd(3); SB(); stat(0); stat(1); stat(2); stat(3);
            wait_vm<16>(); rd(4); rd(5); SB(); stat(4); stat(5);
            wait_vm<8>(); rd(6); rd(7); SB(); stat(6); stat(7);
            wait_vm<0>(); rd(8); rd(9); SB(); stat(8); stat(9);
            const float s = sum_x32(s0 + s1), q = sum_x32(q0 + q1);
            mean1 = s * (1.f / 640.f);
            rs1 = __builtin_amdgcn_rsqf(fmaxf(q * (1.f / 640.f) - mean1 * mean1, 0.f) + 1e-5f);
        }
        tag = (((u64)__builtin_amdgcn_readfirstlane(ep.y) << 32) | __builtin_amdgcn_readfirstlane(ep.x)) + 1;
        STAMP(1);
        bar_lds();
        STAMP(3);
        f32x16 acc[4];
        const Frag frag(l16);
        bar();
        STAMP(40); CSTAMP(50);
        v4u w[6];
#pragma unroll
        for (int u = 0; u < 6; ++u) w[u] = frag(u * 1024);
        SB();
#pragma unroll
        for (int i = 0; i < 40; ++i) {
            if (i == 6 || i == 12 || i == 26) bar();
#pragma unroll
            for (int u = 0; u < 3; ++u) {
                mma32f(i == 0, acc[u], w[(i & 1) * 3 + u], a[i]);
                if (i + 2 < 40) w[(i & 1) * 3 + u] = frag(((i + 2) * 3 + u) * 1024);
            }
        }
        mma_end(acc[0], acc[1], acc[2]);
        STAMP(4); CSTAMP(51);
        lds_wait(L_TG + 32, tag);
        auto centred = [&](int idx0, f32x2 (&z)[10]) {
#pragma unroll
            for (int i = 0; i < 5; ++i) {
                const f32x4 cs = *lp<f32x4>(L_C1 + lh * 320 + (idx0 + 4 * i) * 4);
                const int e = idx0 + 4 * i;
                z[2 * i] = fma2(cs.xy, sp2(-mean1), f32x2{acc[e >> 4][e & 15], acc[e >> 4][(e & 15) + 1]});
                z[2 * i + 1] = fma2(cs.zw, sp2(-mean1), f32x2{acc[e >> 4][(e & 15) + 2], acc[e >> 4][(e & 15) + 3]});
            }
        };
        {
            f32x2 z[10]; unsigned pk[20];
            const int ro = L_PKV + (lh * 128 + 32 * wv + l31) * 144;
            centred(20, z);
#pragma unroll
            for (int i = 0; i < 10; ++i) pk[i] = pk2v(z[i] * rs1);
            lds_st128(ro + 80, v4u{pk[0], pk[1], pk[2], pk[3]});
            lds_st128(ro + 96, v4u{pk[4], pk[5], pk[6], pk[7]});
            lds_st128(ro + 112, v4u{pk[8], pk[9], 0x3f80u, 0u});
            centred(0, z);
            feature_map(z, rs1, pk);
#pragma unroll
            for (int i = 0; i < 5; ++i) lds_st128(ro + i * 16, v4u{pk[4 * i], pk[4 * i + 1], pk[4 * i + 2], pk[4 * i + 3]});
        }
        STAMP(22); CSTAMP(57);
        bar_lds();
        {
            const int head = wv >> 1, ft = wv & 1, blk = lg & 1, q = l15 >> 2, p = lane & 3;
            const int rb = L_PKV + (head * 128 + 8 * lh + q) * 144;
            const int ao = rb + (32 * ft + 16 * blk + 8 * (p & 1) + 4 * (p >> 1)) * 2, bo = rb + (40 + 16 * blk + 4 * p) * 2;
            f32x16 d = {};
            v4u ta[4], tb[4];
#pragma unroll
            for (int s = 0; s < 4; ++s) { ta[s] = lds_tr2(ao + s * 2304, ao + s * 2304 + 576); tb[s] = lds_tr2(bo + s * 2304, bo + s * 2304 + 576); }
            SB();
#pragma unroll
            for (int s = 0; s < 8; ++s) {
                d = mfma32(ta[s & 3], tb[s & 3], d);
                if (s < 4) { ta[s] = lds_tr2(ao + (s + 4) * 2304, ao + (s + 4) * 2304 + 576); tb[s] = lds_tr2(bo + (s + 4) * 2304, bo + (s + 4) * 2304 + 576); }
                SB();
            }
            const v4u c0 = {pk2(d[0], d[1]), pk2(d[2], d[3]), pk2(d[4], d[5]), pk2(d[6], d[7])};
            const v4u c1 = {pk2(d[8], d[9]), pk2(d[10], d[11]), pk2(d[12], d[13]), pk2(d[14], d[15])};
            const unsigned o = OFF_KVP + (hp * 16 + m) * 3584 + ((head * 5 + 4 * ft + lh) * 21 + l31) * 16;
            st128c(c0, rws, (l31 < 21 && (ft == 0 || lh == 0)) ? o : OOB, 0);
            st128c(c1, rws, (l31 < 21 && ft == 0) ? o + 2 * 21 * 16 : OOB, 0);
        }
        STAMP(23);
        unsigned lq = L_W1Q + (lh * 24 + (l31 < 24 ? l31 : l31 - 8)) * 16;
        asm volatile("" : "+v"(lq));
#pragma unroll
        for (int u = 0; u < 6; ++u) w[u] = lds128(lq + u * 768);
        SB();
#pragma unroll
        for (int i = 0; i < 40; ++i) {
            if (i == P1_FLAG_AT) { wait_vm<0>(); arrive4(L_CNT, lane, ws, OFF_FA + (hp * 16 + m) * 128, tag); STAMP(5); }
            mma32f(i == 0, acc[3], w[i % 6], a[i]);
            if (i + 6 < 40) w[i % 6] = lds128(lq + (i + 6) * 768);
        }
        mma_end(acc[3]);
        STAMP(24);
        {
            const rsrc_t rx = mk_rsrc(x, SEQ * DM * 2);
            const unsigned o = ((64 * t + 16 * wv + l15) * 640 + 80 * c + 20 * lg) * 2;
            xr0 = ld128p(rx, o, 0); xr1 = ld128p(rx, o + 16, 0); xr2 = ld64p(rx, o + 32, 0);
        }
        f32x2 z[10];
        centred(40, z);
        feature_map(z, rs1, phq);
#pragma unroll
        for (int i = 0; i < 20; ++i) asm volatile("" : "+v"(phq[i]));
        STAMP(6);
        }
        bar();
        STAMP(7);
        gather();
        bar_lds();
        STAMP(8);
        unsigned hk[10];
        {
            const int i16 = (l31 & 3) + 4 * (l31 >> 3);
            const bool mine = ((l31 >> 2) & 1) == lh;
            const int a0 = mine ? L_KVT + (lh * 105 + i16) * 16 : L_ZR, a1 = mine ? L_KVT + (lh * 105 + 16 + (i16 < 5 ? i16 : 4)) * 16 : L_ZR;
            v4u k0[5], k1[5];
#pragma unroll
            for (int s = 0; s < 5; ++s) { k1[s] = lds128(a1 + s * 336); k0[s] = lds128(a0 + s * 336); }
            SB();
            f32x16 d0 = {}, d1 = {};
#pragma unroll
            for (int s = 0; s < 5; ++s) {
                const v4u bq = {phq[4 * s], phq[4 * s + 1], phq[4 * s + 2], phq[4 * s + 3]};
                d1 = mfma32(k1[s], bq, d1);
                d0 = mfma32(k0[s], bq, d0);
            }
            const float rd = __builtin_amdgcn_rcpf(d1[4] + 1e-6f);
            const v4u c0 = {pk2(d0[0] * rd, d0[1] * rd), pk2(d0[2] * rd, d0[3] * rd), pk2(d0[4] * rd, d0[5] * rd), pk2(d0[6] * rd, d0[7] * rd)};
            const v4u c1 = {pk2(d0[8] * rd, d0[9] * rd), pk2(d0[10] * rd, d0[11] * rd), pk2(d0[12] * rd, d0[13] * rd), pk2(d0[14] * rd, d0[15] * rd)};
            const v2u c2 = {pk2(d1[0] * rd, d1[1] * rd), pk2(d1[2] * rd, d1[3] * rd)};
            const unsigned o = OFF_O + (8 * m + 2 * wv + (l31 >> 4)) * 20480 + 5 * hp * 256 + l15 * 16;
            st128c(c0, rws, o + lh * 512, 0);
            st128c(c1, rws, o + lh * 512 + 256, 0);
            st64c(c2, rws, o + 1024 + lh * 8, 0);
        }
        STAMP(25);
        {
        const Frag frag(l16);
        v4u w[30];
#pragma unroll
        for (int f = 0; f < 30; ++f) w[f] = frag(w2frag(f));
        wait_vm<0>();
        arrive4(L_CNT, lane, ws, OFF_FB + (m * 16 + hp) * 128, tag);
        STAMP(9);
        bar();
        STAMP(10);
        v4u a[20];
        const unsigned oo = OFF_O + (4 * t + wv) * 20480;
        auto old = [&](int i) { a[i] = ld128c(rws, l16, oo + i * 1024); };
        constexpr int PD = P2_PD;
#pragma unroll
        for (int i = 0; i < PD; ++i) old(i);
        SB();
        f32x4 acc[5];
#pragma unroll
        for (int f = 0; f < 100; ++f) {
            if (f % 5 == 0 && f / 5 + PD < 20) old(f / 5 + PD);
            mma16f(f < 5, acc[f % 5], w[f % 30], a[f / 5]);
            if (f + 30 < 100) w[f % 30] = frag(w2frag(f + 30));
        }
        *lp<volatile unsigned>(L_W2D + wv * 4) = 1u;
        mma_end(acc);
        STAMP(26);
        const unsigned xr[10] = {xr0.x, xr0.y, xr0.z, xr0.w, xr1.x, xr1.y, xr1.z, xr1.w, xr2.x, xr2.y};
        float s = 0.f, q = 0.f;
#pragma unroll
        for (int i = 0; i < 10; ++i) {
            hk[i] = pk2(acc[i >> 1][2 * (i & 1)] + bflo(xr[i]), acc[i >> 1][2 * (i & 1) + 1] + bfhi(xr[i]));
            s = dot2(hk[i], 0x3f803f80u, s); q = dot2(hk[i], hk[i], q);
        }
        const unsigned o = OFF_HB + (4 * t + wv) * 20480 + 10 * c * 256 + l15 * 16;
        st128c(v4u{hk[0], hk[1], hk[2], hk[3]}, rws, o + lg * 512, 0);
        st128c(v4u{hk[4], hk[5], hk[6], hk[7]}, rws, o + lg * 512 + 256, 0);
        st64c(v2u{hk[8], hk[9]}, rws, o + (8 + (lg >> 1)) * 256 + (lg & 1) * 8, 0);
        s = sum_x32(sum_x16(s)); q = sum_x32(sum_x16(q));
        st64c(v2u{__float_as_uint(s), __float_as_uint(q)}, rws, lane < 16 ? OFF_ST + ((4 * t + wv) * 8 + c) * 128 + lane * 8 : OOB, 0);
        STAMP(27);
        wait_vm<0>();
        arrive4(L_CNT, lane, ws, OFF_FC + (t * 8 + c) * 128, tag);
        STAMP(11);
        }
        bar();
        STAMP(12); CSTAMP(52);
        if (j < 31) {
            const int tt0 = 16 * TT + 4 * wv;
            const unsigned ho = OFF_HB + tt0 * 20480;
            const Frag frag(l16);
            constexpr int PD = P3_PD;
            v4u a[80], w[7];
            v2u st[2][2][2];
            unsigned rdy = 0;
            auto hld = [&](int f) { if (f < 80) a[f] = ld128c(rws, l16, ho + ((f / 40) * 2 + (f & 1)) * 20480 + ((f % 40) >> 1) * 1024); };
            auto stld = [&](int e) { st[e >> 2][(e >> 1) & 1][e & 1] = ld64c(rws, (2 * lg + (e & 1)) * 128 + l15 * 8, OFF_ST + (tt0 + (e >> 1)) * 1024); };
#pragma unroll
            for (int f = 0; f < 2 * PD; ++f) hld(f);
#pragma unroll
            for (int u = 0; u < 7; ++u) w[u] = frag(w3slot(0) + u * 1024);
#pragma unroll
            for (int ps = 0; ps < 2; ++ps) {
                f32x4 acc[2][7];
#pragma unroll
                for (int i = 0; i < 20; ++i) {
                    if (ps == 0 && (i == 11 || i == 15)) {
                        const unsigned need = i == 11 ? 2u : 3u;
                        if (!__all(rdy >= need)) {
                            for (int sp = 0; sp < (1 << 22); ++sp)
                                if (__all(*lp<volatile unsigned>(L_W3R + (lane & 3) * 4) >= need)) break;
                        }
                        asm volatile("" ::: "memory");
                    }
                    if (ps == 1 && i == 12) *lp<volatile unsigned>(L_PRG + wv * 4) = 1u;
                    if (ps == 1 && i == P3_FLAG_AT) {
                        wait_vm<2 * P3_FLAG_AT>();
                        if (lane == 0) set_flag(ws, OFF_FD + (((4 * TT + wv) * 2) * 32 + j) * 128, tag);
                    }
#pragma unroll
                    for (int u = 0; u < 7; ++u) {
                        mma16f(i == 0, acc[0][u], w[u], a[40 * ps + 2 * i]);
                        if (u == 2 || u == 5) hld(40 * ps + 2 * (i + PD) + (u == 5));
                        if (u == 0 && i >= 8 && i < 12) stld(4 * ps + i - 8);
                        if (ps == 0 && u == 3 && (i == 10 || i == 14)) rdy = *lp<volatile unsigned>(L_W3R + (lane & 3) * 4);
                        mma16f(i == 0, acc[1][u], w[u], a[40 * ps + 2 * i + 1]);
                        if (i + 1 < 20 || ps == 0) w[u] = frag(w3slot((i + 1) % 20) + u * 1024);
                    }
                }
                mma_end(acc[0]); mma_end(acc[1]);
                if (ps == 0) { STAMP(32); CSTAMP(53); } else { STAMP(13); CSTAMP(58); }
                f32x4 cs[7];
#pragma unroll
                for (int u = 0; u < 7; ++u) cs[u] = *lp<f32x4>(L_CS + (u * 16 + lg * 4) * 4);
                if (ps == 1) bar_lds();
                unsigned hh[2][7];
                p3_epilogue(acc, cs, st[ps], hh);
                const unsigned o = OFF_HH + (tt0 + 2 * ps) * 55296 + 7 * j * 256 + l15 * 16;
                st128c(v4u{hh[0][0], hh[0][1], hh[0][2], hh[0][3]}, rws, o + lg * 256, 0);
                st128c(v4u{hh[1][0], hh[1][1], hh[1][2], hh[1][3]}, rws, o + lg * 256, 55296);
                swap16(hh[0][4], hh[1][4]); swap16(hh[0][5], hh[1][5]); swap16(hh[0][6], hh[1][6]);
                st128c(v4u{hh[0][4], hh[0][5], hh[1][4], hh[1][5]}, rws, o + (lg & 1) * 55296 + (4 + (lg >> 1)) * 256, 0);
                st64c(v2u{hh[0][6], hh[1][6]}, rws, j < 30 ? o + (lg & 1) * 55296 + 6 * 256 + (lg >> 1) * 8 : OOB, 0);
                asm volatile("" ::: "memory");
                if (ps == 0) { STAMP(33); CSTAMP(54); } else { STAMP(29); CSTAMP(59); }
            }
            bar();
            wait_vm<0>();
            if (lane == 0) set_flag(ws, OFF_FD + (((4 * TT + wv) * 2 + 1) * 32 + j) * 128, tag);
            STAMP(14);
        } else {
            bar();
            bar();
        }
        if (wv == 0) {
            __builtin_amdgcn_s_sleep(D_SLEEP);
            poll(ws, OFF_FD + ((t * 2 + 1) * 32 + (l31 < 31 ? l31 : 30)) * 128, tag);
        }
        bar();
        bar();
        asm volatile("fb_code_hi:");
    } else {
        {
            const int isq = (wv - 4) >> 1, fhead = wv & 1, lf = L_F + (wv - 4) * 5120;
            if (P0_PRIO) __builtin_amdgcn_s_setprio(P0_PRIO);
            const rsrc_t rq = mk_rsrc(wqkv, DM * 3 * DM * 2), rp = mk_rsrc(wphi, 32 * 400 * 2);
            const unsigned so = (40 * m * 1920 + (2 * hp + fhead) * 20) * 2;
            if (lane < 63) {
                const unsigned vo = (lane / 3) * 3840 + (lane % 3) * 16;
                dma_p(rq, lf, vo, so + (isq ? 0 : 1280)); dma_p(rq, lf + 1008, lane < 57 ? vo : OOB, so + (isq ? 0 : 1280) + 21 * 3840);
                dma_p(rp, lf + 2016, lane < 50 ? l16 : OOB, (2 * hp + fhead) * 800);
                dma_p(rq, lf + 3024, (isq && lane >= 48) ? OOB : vo, so + 2560 + (isq ? 24 * 3840 : 0));
                dma_p(rq, lf + 4032, (isq || lane >= 9) ? OOB : vo, so + 2560 + 21 * 3840);
            }
            const int r = (l31 & 3) + 4 * (l31 >> 3), hh = (l31 >> 2) & 1;
            const int a1 = lf + (16 * (r >> 3) + 8 * hh + (r & 7)) * 48, a2 = lf + (32 + (r & 7)) * 48;
            const int tc = (16 * (lg & 1) + 4 * (lane & 3)) * 2;
            const int pa = lf + 2016 + (l15 >> 2) * 40 + tc, va = lf + 3024 + (8 * lh + (l15 >> 2)) * 48 + tc;
            const unsigned ob = OFF_W1 + hp * 163840 + (5 * m + lh) * 2048 + fhead * 1024 + l31 * 16;
            const unsigned o = l31 < 20 ? ob + (isq ? 640 : 0) : OOB, ov = l31 < 20 ? ob + 320 + (isq ? 6144 : 0) : OOB;
            STAMP(20);
            wait_vm<0>();
            STAMP(21);
            const v4u fa0 = lds128(a1 + 16 * lh), fc0 = lds128(a2 + 16 * lh);
            const v2u fa1 = *lp<v2u>(a1 + 32), fc1 = *lp<v2u>(a2 + 32);
            const v4u b0 = lds_tr2(pa + lh * 320, pa + lh * 320 + 160);
            const v2u p2 = lds_tr(pa + 640);
            const v4u v0 = lds_tr2(va, va + 192), v1 = lds_tr2(va - lh * 384 + 768, va - lh * 384 + 960);
            const v4u b1 = {lh == 0 ? p2.x : 0u, lh == 0 ? p2.y : 0u, 0u, 0u};
            f32x16 d = {}, e = {};
            d = mfma32(fa0, b0, d);
            e = mfma32(fc0, b0, e);
            d = mfma32(v4u{fa1.x, fa1.y, 0u, 0u}, b1, d);
            e = mfma32(v4u{fc1.x, fc1.y, 0u, 0u}, b1, e);
            const bool third = lh == 0 && !isq;
            st128c(v0, rws, ov, 0);
            st128c(v1, rws, third ? ov : OOB, 4096);
            const float sv = sum_x32(sum8(v0, third ? sum8(v1, 0.f) : 0.f));
            const float sc = LOG2E;
            const v4u c0 = {pk2(d[0] * sc, d[1] * sc), pk2(d[2] * sc, d[3] * sc), pk2(d[4] * sc, d[5] * sc), pk2(d[6] * sc, d[7] * sc)};
            const v4u c1 = {pk2(d[8] * sc, d[9] * sc), pk2(d[10] * sc, d[11] * sc), pk2(d[12] * sc, d[13] * sc), pk2(d[14] * sc, d[15] * sc)};
            const v4u c2 = {pk2(e[0] * sc, e[1] * sc), pk2(e[2] * sc, e[3] * sc), pk2(e[4] * sc, e[5] * sc), pk2(e[6] * sc, e[7] * sc)};
            st128c(c0, rws, o, 0);
            st128c(c1, rws, o, 4096);
            st128c(c2, rws, lh == 0 ? o : OOB, 8192);
            const float sw = sum_x32(sum8(c0, sum8(c1, lh == 0 ? sum8(c2, 0.f) : 0.f)));
            st32c(__float_as_uint(lh == 0 ? sw : sv), rws, l31 < 20 ? OFF_C1P + (hp * 16 + m) * 640 + fhead * 320 + (isq * 40 + lh * 20 + l31) * 4 : OOB, 0);
        }
        tag = (((u64)__builtin_amdgcn_readfirstlane(ep.y) << 32) | __builtin_amdgcn_readfirstlane(ep.x)) + 1;
        STAMP(1);
        wait_vm<0>();
        *lp<volatile u64>(L_TG + (wv - 4) * 8) = tag;
        if (wv == 4) {
            lds_wait(L_TG + (lane & 3) * 8, tag);
            if (lane == 0) set_flag(ws, OFF_F0 + (hp * 16 + m) * 128, tag);
            STAMP(2);
            __builtin_amdgcn_s_sleep(P0_DELAY);
            poll(ws, OFF_F0 + (hp * 16 + l15) * 128, tag);
        }
        if (P0_PRIO) __builtin_amdgcn_s_setprio(0);
        bar();
        STAMP(3);
        const unsigned wb1 = OFF_W1 + hp * 163840;
        if (wv < 6) {
            const int lw = wv - 4, i16 = (l31 & 3) + 4 * (l31 >> 3);
            const unsigned wl = lh * 2048 + ((l31 >> 2) & 1) * 1024 + i16 * 16;
            auto issue = [&](int ch) {
#pragma unroll
                for (int u = 0; u < 6; ++u) dma_c(rws, ((4 * ch + 2 * lw) * 3 + u) * 1024, wl, wb1 + (4 * ch + 2 * lw + u / 3) * 4096 + (u % 3) * 256);
            };
            issue(0); issue(1); issue(2); issue(3); wait_vm<12>(); bar();
            issue(4); issue(5); issue(6); wait_vm<18>(); bar();
            issue(7); issue(8); issue(9); wait_vm<18>(); bar();
            wait_vm<0>(); bar();
            {
                const int r3 = lane < 24 ? lane : lane - 24;
                const unsigned wl3 = (lane < 24 ? 0 : 2048) + ((r3 >> 2) & 1) * 1024 + (48 + (r3 & 3) + 4 * (r3 >> 3)) * 16;
                if (lane < 48) {
#pragma unroll
                    for (int i = 0; i < 20; ++i) dma_c(rws, L_W1Q + (2 * i + lw) * 768, wl3, wb1 + (2 * i + lw) * 4096);
                }
                wait_vm<0>();
            }
            STAMP(22);
            bar();
            __builtin_amdgcn_s_sleep(A_SLEEP);
            poll(ws, OFF_FWC + (c * 32 + l15) * 128, tag);
            STAMP(23);
            dma_run<40>(rws, lw * 77824, l16, OFF_W2 + c * 102400 + lw * 40960);
            dma_run<2>(rws, w2frag(80) + lw * 2048, l16, OFF_W2 + c * 102400 + 81920 + lw * 2048);
            STAMP(24);
        } else {
            bar();
            if (wv == 6) {
                v4u cp[16];
#pragma unroll
                for (int i = 0; i < 16; ++i) cp[i] = ld128c(rws, lane < 40 ? l16 : OOB, OFF_C1P + (hp * 16 + i) * 640);
                bar();
#pragma unroll
                for (int i = 0; i < 16; ++i) asm volatile("" : "+v"(cp[i]));
                bar();
                const rsrc_t rg = mk_rsrc(wg, DM * FF * 2), ru = mk_rsrc(wu, DM * FF * 2);
                const unsigned vo = (lane / 7) * (FF * 2) + (lane % 7) * 16, so = (80 * TT * FF + 56 * j) * 2;
                if (lane < 63) {
#pragma unroll
                    for (int i = 0; i < 9; ++i) { dma_p(rg, L_RG + i * 1008, vo, so + i * 9 * FF * 2); dma_p(ru, L_RU + i * 1008, vo, so + i * 9 * FF * 2); }
                }
                float c1s[4];
#pragma unroll
                for (int e = 0; e < 4; ++e) { const unsigned u = cp[0][e]; c1s[e] = __uint_as_float(u); }
#pragma unroll
                for (int i = 1; i < 16; ++i) {
                    const unsigned u0 = cp[i].x, u1 = cp[i].y, u2 = cp[i].z, u3 = cp[i].w;
                    asm("v_add_f32 %0, %0, %4\n\tv_add_f32 %1, %1, %5\n\tv_add_f32 %2, %2, %6\n\tv_add_f32 %3, %3, %7"
                        : "+v"(c1s[0]), "+v"(c1s[1]), "+v"(c1s[2]), "+v"(c1s[3]) : "v"(u0), "v"(u1), "v"(u2), "v"(u3));
                }
#pragma unroll
                for (int e = 0; e < 4; ++e) {
                    float o2 = __shfl(c1s[e], lane + 10);
                    o2 = ((lane >= 5 && lane < 10) || (lane >= 25 && lane < 30)) ? o2 : 0.f;
                    asm("v_add_f32 %0, %0, %1" : "+v"(c1s[e]) : "v"(o2));
                }
                if (lane < 40) *lp<f32x4>(L_C1 + l16) = f32x4{c1s[0], c1s[1], c1s[2], c1s[3]};
                wait_lds();
                *lp<volatile u64>(L_TG + 32) = tag;
            } else {
                bar(); bar();
                const rsrc_t rd = mk_rsrc(wd, DM * FF * 2), ro = mk_rsrc(wo, DM * DM * 2);
                const unsigned vo = (lane / 10) * (DM * 2) + (lane % 10) * 16;
                if (lane < 60) {
#pragma unroll
                    for (int i = 0; i < 10; ++i) dma_p(rd, L_RD + i * 960, vo, (56 * t * DM + 80 * c) * 2 + i * 6 * DM * 2);
#pragma unroll
                    for (int i = 0; i < 7; ++i) dma_p(ro, L_RO + i * 960, vo, (40 * t * DM + 80 * c) * 2 + i * 6 * DM * 2);
                }
            }
            STAMP(41);
            bar();
            const int q = l15 >> 2, p = lane & 3;
            v4u rt[18];
            unsigned dv[18];
            wait_vm<0>();
            STAMP(42);
            if (wv == 6) {
                const int ra = L_RG + (20 * lg + q) * 112, c6 = (p >> 1) * 9216 + (48 + 4 * (p & 1)) * 2;
                const int rb = L_RG + (40 * (lg & 1) + 16 + q) * 112;
#pragma unroll
                for (int hf = 0; hf < 2; ++hf) {
#pragma unroll
                    for (int ti = 0; ti < 6; ++ti) { const int a = ra + 24 * p + hf * 896 + (ti / 3) * 9216 + (ti % 3) * 8; rt[hf * 7 + ti] = lds_tr2(a, a + 448); }
                    rt[hf * 7 + 6] = lds_tr2(ra + c6 + hf * 896, ra + c6 + hf * 896 + 448);
                }
#pragma unroll
                for (int s = 0; s < 3; ++s) { const int a = rb + (lg >> 1) * 9216 + 24 * p + s * 8; rt[14 + s] = lds_tr2(a, a + 2240); }
                rt[17] = lds_tr2(rb + c6, rb + c6 + 2240);
                const bool on = j < 31;
                auto dst = [&](int Q) { return OFF_W3 + (j * 20 + (Q >> 2)) * 7168 + ((Q & 3) * 16 + l15) * 16; };
                const unsigned d0 = on ? dst(10 * TT + 2 * lg) : OOB, d1 = on ? dst(10 * TT + 2 * lg + 1) : OOB, d2 = on ? dst(10 * TT + 8 + (lg & 1)) : OOB;
#pragma unroll
                for (int ti = 0; ti < 7; ++ti) { dv[ti] = d0 + ti * 1024; dv[7 + ti] = d1 + ti * 1024; }
#pragma unroll
                for (int s = 0; s < 3; ++s) dv[14 + s] = d2 + (lg >> 1) * 3072 + s * 1024;
                dv[17] = lg < 2 ? d2 + 6144 : OOB;
            } else {
                const int ra = L_RD + (12 * lg + q) * 160 + 40 * p;
                const int r0 = lg < 2 ? 24 * lg + 8 + q : 48 + 4 * (q >> 1) + (q & 1), r1 = lg < 2 ? r0 + 12 : r0 + 2;
                const int oa = L_RO + (20 * (lg >> 1) + 8 * (lg & 1) + q) * 160 + 40 * p, ob = L_RO + (16 + q) * 160 + 40 * p;
                auto d4 = [&](int Q) { return Q < 216 ? OFF_W4 + (c * 54 + (Q >> 2)) * 5120 + ((Q & 3) * 16 + l15) * 16 : OOB; };
                auto d2 = [&](int Q) { return t < 16 ? OFF_W2 + (c * 20 + (Q >> 2)) * 5120 + ((Q & 3) * 16 + l15) * 16 : OOB; };
                const unsigned da = d4(7 * t + lg), db = lg < 3 ? d4(7 * t + 4 + lg) : OOB, dc = d2(5 * t + lg), dd = d2(5 * t + 4);
#pragma unroll
                for (int ti = 0; ti < 5; ++ti) {
                    rt[ti] = lds_tr2(ra + ti * 8, ra + ti * 8 + 640);
                    rt[5 + ti] = lds_tr2(L_RD + r0 * 160 + 40 * p + ti * 8, L_RD + r1 * 160 + 40 * p + ti * 8);
                    rt[10 + ti] = lds_tr2(oa + ti * 8, oa + ti * 8 + 640);
                    dv[ti] = da + ti * 1024; dv[5 + ti] = db + ti * 1024; dv[10 + ti] = dc + ti * 1024;
                }
                rt[15] = lds_tr2(ob + lg * 8, ob + lg * 8 + 3200);
                rt[16] = lds_tr2(ob + 32, ob + 32 + 3200);
                rt[17] = rt[16];
                dv[15] = dd + lg * 1024; dv[16] = lg == 0 ? dd + 4096 : OOB; dv[17] = OOB;
            }
            STAMP(43);
#pragma unroll
            for (int i = 17; i >= 0; --i) st128c(rt[i], rws, dv[i], 0);
            STAMP(44);
            bar();
            if (wv == 6) {
                float cs[8];
#pragma unroll
                for (int ti = 0; ti < 7; ++ti) cs[ti] = sum8(rt[ti], sum8(rt[7 + ti], 0.f));
#pragma unroll
                for (int s = 0; s < 3; ++s) { const float e = sum8(rt[14 + s], 0.f); cs[s] += lg < 2 ? e : 0.f; cs[s + 3] += lg < 2 ? 0.f : e; }
                { const float e = sum8(rt[17], 0.f); cs[6] += lg < 2 ? e : 0.f; }
#pragma unroll
                for (int ti = 0; ti < 7; ++ti) cs[ti] = sum_x32(sum_x16(cs[ti]));
                cs[7] = 0.f;
                const float ca = lg == 0 ? cs[0] : lg == 1 ? cs[1] : lg == 2 ? cs[2] : cs[3], cb = lg == 0 ? cs[4] : lg == 1 ? cs[5] : lg == 2 ? cs[6] : cs[7];
                st32c(__float_as_uint(ca), rws, lane * 4, OFF_CSP + (j * 8 + TT) * 512);
                st32c(__float_as_uint(cb), rws, lane * 4 + 256, OFF_CSP + (j * 8 + TT) * 512);
                wait_vm<0>();
                if (lane == 0) set_flag(ws, OFF_FWJ + (j * 8 + TT) * 128, tag);
            } else {
                wait_vm<10>();
                if (lane == 0) set_flag(ws, OFF_FWC + (c * 32 + t) * 128, tag);
                wait_vm<1>();
                if (lane == 0) set_flag(ws, OFF_FW4 + (c * 32 + t) * 128, tag);
                lds_wait_ge(L_CNT, 4u);
                __builtin_amdgcn_s_sleep(PA_DELAY);
                poll(ws, OFF_FA + (hp * 16 + l15) * 128, tag);
            }
            STAMP(22);
        }
        bar();
        STAMP(7);
        if (wv < 7) {
            gather();
        } else {
            if (lane < 5) lds_st128(L_ZR + lane * 336, v4u{0u, 0u, 0u, 0u});
            poll(ws, lane < 32 ? OFF_FW4 + (c * 32 + lane) * 128 : OFF_FWJ + (j * 8 + (lane & 7)) * 128, tag);
        }
        bar_lds();
        STAMP(8);
        const int hw = wv - 4;
        const int k0 = hw < 2 ? 14 * hw : 28 + 13 * (hw - 2), sk = hw < 2 ? 0 : 1;
        const unsigned wb4 = OFF_W4 + c * 276480 + k0 * 5120, hb = OFF_HH + 4 * t * 55296 + k0 * 1024;
        const unsigned l16x = hw < 2 ? l16 : OOB;
        const int le = hw < 3 ? hw * 20480 : 122880, ll = hw < 3 ? 61440 + hw * 20480 : 143360;
        unsigned leb = le + l16, llb = ll + l16;
        asm volatile("" : "+v"(leb), "+v"(llb));
        v4u wr[6][5], wl[20], hr0, hr1; v2u hr2;
        auto w4ld = [&](auto f0, auto f1, auto pace) {
#pragma unroll
            for (int f = decltype(f0)::value; f < decltype(f1)::value; ++f) {
                if (f < 30) wr[f / 5][f % 5] = ld128c(rws, f >= 25 ? l16x : l16, wb4 + f * 1024);
                else wl[f - 30] = ld128c(rws, l16, wb4 + (10 - sk) * 5120 + (f - 30) * 1024);
                asm volatile("s_sleep %0" ::"n"(decltype(pace)::value) : "memory");
            }
        };
        __builtin_amdgcn_s_sleep(B_SLEEP);
        {
            const unsigned v3 = j < 31 ? l16 : OOB, w3b = OFF_W3 + j * 143360;
            if (hw == 0) { dma_run<15>(rws, w2frag(84), l16, OFF_W2 + c * 102400 + 86016); dma_c(rws, w2frag(99), l16, OFF_W2 + c * 102400 + 101376); }
            else if (hw == 1) dma_run<21>(rws, w3slot(0), v3, w3b);
            else dma_run<14>(rws, w3slot(3) + (hw - 2) * 14336, v3, w3b + 21504 + (hw - 2) * 14336);
        }
        w4ld(ic<0>{}, ic<W4_N1>{}, ic<0>{});
        if (hw == 3) {
            lds_wait_ge(L_CNT, 8u);
            __builtin_amdgcn_s_sleep(PB_DELAY);
            poll(ws, OFF_FB + ((t >> 1) * 16 + l15) * 128, tag);
            if (b == 0 && lane == 0) set_flag(ws, OFF_EPOCH, tag);
        } else {
            wait_vm<W4_N1>();
        }
        STAMP(26);
        bar();
        if (hw == 3) {
            float s0 = 0.f, s1 = 0.f;
#pragma unroll
            for (int i = 0; i < 8; ++i) {
                s0 += __uint_as_float(ld32c(rws, lane * 4, OFF_CSP + (j * 8 + i) * 512));
                s1 += __uint_as_float(ld32c(rws, lane * 4 + 256, OFF_CSP + (j * 8 + i) * 512));
            }
            *lp<float>(L_CS + lane * 4) = s0;
            if (lane < 48) *lp<float>(L_CS + 256 + lane * 4) = s1;
            __builtin_amdgcn_s_sleep(P2_SLEEP / 2);
        } else {
            __builtin_amdgcn_s_sleep(P2_SLEEP);
        }
        w4ld(ic<W4_N1>{}, ic<W4_N1 + W4_N2>{}, ic<0>{});
        lds_wait_ge(L_W2D + (lane & 3) * 4, 1u);
        const unsigned v3 = j < 31 ? l16 : OOB, w3r = OFF_W3 + j * 143360 + 50176 + hw * 1024;
        auto w3rest = [&](int i) {
            if (i < 9) { if (i < 8 || hw < 3) dma_c(rws, w3slot(7) + hw * 1024 + i * 4096, v3, w3r + i * 4096); }
            else dma_c(rws, w3slot(12) + hw * 1024 + (i - 9) * 4096, v3, w3r + 35840 + (i - 9) * 4096);
        };
#pragma unroll
        for (int i = 0; i < 9; ++i) w3rest(i);
        wait_vm<0>();
        STAMP(36);
        if (hw == 3) {
            lds_wait_ge(L_CNT, 12u);
            __builtin_amdgcn_s_sleep(PC_DELAY);
            poll(ws, OFF_FC + (TT * 32 + l31) * 128, tag);
        }
        STAMP(28);
        bar_lds();
        STAMP(12);
        {
            __builtin_amdgcn_s_sleep(W4_SLEEP);
#pragma unroll
            for (int i = 9; i < 23; ++i) {
                w3rest(i);
                asm volatile("s_sleep %0" ::"n"(W3_PACE) : "memory");
                if (i == 20) { wait_vm<5>(); *lp<volatile unsigned>(L_W3R + hw * 4) = 2u; }
            }
            wait_vm<0>();
            *lp<volatile unsigned>(L_W3R + hw * 4) = 3u;
            STAMP(35);
            w4ld(ic<W4_N1 + W4_N2>{}, ic<50>{}, ic<W4_PACE>{});
            const unsigned ho = OFF_HB + (4 * t + hw) * 20480 + 10 * c * 256 + l15 * 16;
            hr0 = ld128c(rws, ho + lg * 512, 0); hr1 = ld128c(rws, ho + lg * 512 + 256, 0); hr2 = ld64c(rws, ho + (8 + (lg >> 1)) * 256 + (lg & 1) * 8, 0);
            STAMP(34);
            if (j < 31) lds_wait_ge(L_PRG + (lane & 3) * 4, 1u);
            dma_run<20>(rws, le, l16, wb4 + (6 - sk) * 5120);
            if (hw == 3) poll(ws, OFF_FD + ((t * 2 + 0) * 32 + (l31 < 31 ? l31 : 30)) * 128, tag);
            STAMP(31);
            bar();
            STAMP(15);
#pragma unroll
            for (int f = 0; f < 20; ++f) lds_st128(llb + f * 1024, wl[f]);
            wait_vm<0>();
        }
        STAMP(30);
        f32x4 acc[2][5];
        v4u fh[5];
        auto run = [&](auto kc, auto pdc) {
            constexpr int k = decltype(kc)::value, PD = decltype(pdc)::value;
            v4u a[28], w[5];
            auto hld = [&](int f) {
                const int n = f >> 1;
                if (f < 28) a[f] = ld128c(rws, n == 5 ? l16x : l16, hb + (k + (f & 1)) * 55296 + (n < 6 ? n : n - sk) * 1024);
            };
#pragma unroll
            for (int f = 0; f < 2 * PD; ++f) hld(f);
            if (k == 0) bar();
#pragma unroll
            for (int n = 0; n < 14; ++n) {
#pragma unroll
                for (int u = 0; u < 5; ++u) {
                    const v4u wf = n < 6 ? wr[n < 6 ? n : 0][u] : w[u];
                    mma16f(n == 0, acc[0][u], wf, a[2 * n]);
                    if (n > 0 && (u == 0 || u == 2)) hld(2 * (n - 1 + PD) + (u == 2));
                    mma16f(n == 0, acc[1][u], wf, a[2 * n + 1]);
                    if (n >= 5 && n < 13) { const int f = (n - 5) * 5 + u; w[u] = f < 20 ? lds128(leb + f * 1024) : lds128(llb + (f - 20) * 1024); }
                    if (k == 2 && n == 10) lds_st128(leb + u * 1024, fh[u]);
                }
            }
            mma_end(acc[0]); mma_end(acc[1]);
        };
        run(ic<0>{}, ic<P4_PD1>{});
#pragma unroll
        for (int u = 0; u < 5; ++u) {
            fh[u] = v4u{pk2v(acc[0][u].xy), pk2v(acc[0][u].zw), pk2v(acc[1][u].xy), pk2v(acc[1][u].zw)};
            asm volatile("" : "+v"(fh[u]));
        }
        STAMP(16);
        bar();
        STAMP(17); CSTAMP(55);
        run(ic<2>{}, ic<P4_PD2>{});
        STAMP(18); CSTAMP(56);
#pragma unroll
        for (int u = 0; u < 5; ++u) lds_st128(leb + (5 + u) * 1024, v4u{pk2v(acc[0][u].xy), pk2v(acc[0][u].zw), pk2v(acc[1][u].xy), pk2v(acc[1][u].zw)});
        bar_lds();
        {
            const unsigned hk[10] = {hr0.x, hr0.y, hr0.z, hr0.w, hr1.x, hr1.y, hr1.z, hr1.w, hr2.x, hr2.y};
            const int ra = (hw >> 1) * 5120 + l16 + (hw & 1) * 8;
            unsigned y[10];
#pragma unroll
            for (int u = 0; u < 5; ++u) {
                f32x2 r0 = {bflo(hk[2 * u]), bfhi(hk[2 * u])}, r1 = {bflo(hk[2 * u + 1]), bfhi(hk[2 * u + 1])};
#pragma unroll
                for (int k = 0; k < 4; ++k) {
                    const v2u p = *lp<v2u>(ra + (k < 3 ? k * 20480 : 122880) + u * 1024);
                    r0 += f32x2{bflo(p.x), bfhi(p.x)}; r1 += f32x2{bflo(p.y), bfhi(p.y)};
                }
                y[2 * u] = pk2v(r0); y[2 * u + 1] = pk2v(r1);
            }
            const rsrc_t ro = mk_rsrc(out, SEQ * DM * 2);
            const unsigned o = ((64 * t + 16 * hw + l15) * 640 + 80 * c + 20 * lg) * 2;
            __builtin_amdgcn_raw_buffer_store_b128(v4u{y[0], y[1], y[2], y[3]}, ro, o, 0, 2);
            __builtin_amdgcn_raw_buffer_store_b128(v4u{y[4], y[5], y[6], y[7]}, ro, o + 16, 0, 2);
            __builtin_amdgcn_raw_buffer_store_b64(v2u{y[8], y[9]}, ro, o + 32, 0, 2);
        }
        STAMP(19);
    }
    STAMP(61);
    if (b == 0 && wv == 0) { const u64 t0 = __builtin_amdgcn_s_memrealtime(); u64 dl = 8 * (ampb - ampa); if (dl > 100000) dl = 100000; while (__builtin_amdgcn_s_memrealtime() - t0 < dl) {} }
}

}

extern "C" void kernel_launch(void* const* d_in, const int* in_sizes, int n_in, void* d_out, int out_size, void* d_ws, size_t ws_size,
                              hipStream_t stream) {
    (void)in_sizes; (void)n_in; (void)out_size; (void)ws_size;
    fused_block<<<256, 512, 0, stream>>>((const u16*)d_in[0], (const u16*)d_in[1], (const u16*)d_in[2], (const u16*)d_in[3],
                                         (const u16*)d_in[5], (const u16*)d_in[6], (const u16*)d_in[7], (u16*)d_out, (char*)d_ws);
}
```

```cpp
#define AMP_A 33
#define AMP_B 13
#include <hip/hip_runtime.h>
#include <stdint.h>

typedef unsigned short u16;
typedef unsigned long long u64;
typedef unsigned v4u __attribute__((ext_vector_type(4)));
typedef unsigned v2u __attribute__((ext_vector_type(2)));
typedef float f32x2 __attribute__((ext_vector_type(2)));
typedef float f32x4 __attribute__((ext_vector_type(4)));
typedef float f32x16 __attribute__((ext_vector_type(16)));
typedef __bf16 bf16x2 __attribute__((ext_vector_type(2)));
typedef __bf16 bf16x8 __attribute__((ext_vector_type(8)));
typedef short s16x4 __attribute__((ext_vector_type(4)));
typedef __amdgpu_buffer_rsrc_t rsrc_t;
#define GAS __attribute__((address_space(1)))
#define LAS __attribute__((address_space(3)))
#define DI __device__ __forceinline__

namespace {

constexpr int SEQ = 2048, DM = 640, FF = 1728;

constexpr unsigned OFF_EPOCH = 0;
constexpr unsigned OFF_F0  = 4096;
constexpr unsigned OFF_FA  = OFF_F0 + 32768;
constexpr unsigned OFF_FB  = OFF_FA + 32768;
constexpr unsigned OFF_FC  = OFF_FB + 32768;
constexpr unsigned OFF_FWC = OFF_FC + 32768;
constexpr unsigned OFF_FWJ = OFF_FWC + 32768;
constexpr unsigned OFF_FW4 = OFF_FWJ + 32768;
constexpr unsigned OFF_FD  = OFF_FW4 + 32768;
constexpr unsigned OFF_W1  = OFF_FD + 262144;
constexpr unsigned OFF_C1P = OFF_W1 + 16 * 163840;
constexpr unsigned OFF_KVP = OFF_C1P + 256 * 640;
constexpr unsigned OFF_O   = OFF_KVP + 256 * 3584;
constexpr unsigned OFF_W2  = OFF_O + 128 * 20480;
constexpr unsigned OFF_HB  = OFF_W2 + 8 * 100 * 1024;
constexpr unsigned OFF_ST  = OFF_HB + 128 * 20480;
constexpr unsigned OFF_CSP = OFF_ST + 128 * 8 * 128;
constexpr unsigned OFF_W3  = OFF_CSP + 32 * 8 * 512;
constexpr unsigned OFF_HH  = OFF_W3 + 31 * 140 * 1024;
constexpr unsigned OFF_W4  = OFF_HH + 128 * 216 * 256;
constexpr unsigned WS_USED = OFF_W4 + 8 * 270 * 1024;
constexpr unsigned OFF_STAMP = (WS_USED + 4095) & ~4095u;
constexpr unsigned OOB = 0x7ffffff0u;

constexpr int LDS_BYTES = 163840;
constexpr int L_X    = 0;
constexpr int L_F    = 131072;
constexpr int L_PF   = 152576;
constexpr int L_RG = 0, L_RU = L_RG + 9216, L_RD = L_RU + 9216, L_RO = L_RD + 9600;
constexpr int L_W1Q  = 122880;
constexpr int L_PKV  = 40960;
constexpr int L_KVT  = 153600;
constexpr int L_ZR   = L_KVT + 3584;
constexpr int L_CS   = L_ZR + 1376;
constexpr int L_C1   = L_CS + 512;
constexpr int L_CNT  = 163584;
constexpr int L_PRG = L_CNT + 16, L_W3R = L_CNT + 32, L_W2D = L_CNT + 48, L_TG = L_CNT + 64;
__host__ __device__ constexpr int w2frag(int f) { return f < 40 ? f * 1024 : f < 84 ? 77824 + (f - 40) * 1024 : f < 99 ? 62464 + (f - 84) * 1024 : 151552; }
__host__ __device__ constexpr int w3slot(int i) { return i < 3 ? 40960 + i * 7168 : i < 7 ? 122880 + (i - 3) * 7168 : i < 12 ? (i - 7) * 7168 : 62464 + (i - 12) * 7168; }

#define CODEPF 0
#define RAW_SLEEP 0
#define W4_SLEEP 12
#define P0_DELAY 12
#define PA_DELAY 14
#define PB_DELAY 14
#define PC_DELAY 14
#define W4_N1 0
#define W4_N2 10
#define D_SLEEP 14
#define RT_SLEEP 6
#define W3_PACE 1
#define W4_PACE 2
#define P3_FLAG_AT 4
#define P3_LPS 4
#define P4_PD1 6
#define P4_PD2 6
#define MAPY 1
#define ROT 0
#define P0_PRIO 3
#define XRING 6
#define A_SLEEP 12
#define B_SLEEP 0
#define C_SLEEP 4
#define P3_PDB 18
#define P3_PACE 4
#define P1_FLAG_AT 20
#define P2_PD 12
#define P2_SLEEP 24
#define P3_PD 7
#define P3_SLEEP 40
#define POLL_SLEEP 1
constexpr int AUX_SC1 = 16;
constexpr float LOG2E = 1.4426950408889634f;

DI rsrc_t mk_rsrc(const void* p, unsigned bytes) { return __builtin_amdgcn_make_buffer_rsrc((void*)p, 0, bytes, 0x00020000); }
DI v4u ld128c(rsrc_t r, unsigned v, unsigned s) { return __builtin_amdgcn_raw_buffer_load_b128(r, v, s, AUX_SC1); }
DI v2u ld64c(rsrc_t r, unsigned v, unsigned s) { return __builtin_amdgcn_raw_buffer_load_b64(r, v, s, AUX_SC1); }
DI unsigned ld32c(rsrc_t r, unsigned v, unsigned s) { return __builtin_amdgcn_raw_buffer_load_b32(r, v, s, AUX_SC1); }
DI v4u ld128p(rsrc_t r, unsigned v, unsigned s) { return __builtin_amdgcn_raw_buffer_load_b128(r, v, s, 0); }
DI v2u ld64p(rsrc_t r, unsigned v, unsigned s) { return __builtin_amdgcn_raw_buffer_load_b64(r, v, s, 0); }
DI void st128c(v4u d, rsrc_t r, unsigned v, unsigned s) { __builtin_amdgcn_raw_buffer_store_b128(d, r, v + s, 0, AUX_SC1); }
DI void st64c(v2u d, rsrc_t r, unsigned v, unsigned s) { __builtin_amdgcn_raw_buffer_store_b64(d, r, v, s, AUX_SC1); }
DI void st32c(unsigned d, rsrc_t r, unsigned v, unsigned s) { __builtin_amdgcn_raw_buffer_store_b32(d, r, v, s, AUX_SC1); }

DI void dma_c(rsrc_t r, unsigned lds, unsigned v, unsigned s) {
    asm volatile("s_mov_b32 m0, %0\n\ts_nop 0\n\tbuffer_load_dwordx4 %1, %2, %3 offen sc1 lds" ::"s"(lds), "v"(v), "s"(r), "s"(s) : "memory");
}
DI void dma_p(rsrc_t r, unsigned lds, unsigned v, unsigned s) {
    asm volatile("s_mov_b32 m0, %0\n\ts_nop 0\n\tbuffer_load_dwordx4 %1, %2, %3 offen lds" ::"s"(lds), "v"(v), "s"(r), "s"(s) : "memory");
}
template <int N> DI void dma_run(rsrc_t r, unsigned lds, unsigned v, unsigned s) {
    if constexpr (N >= 4) {
        asm volatile("s_mov_b32 m0, %0\n\ts_nop 0\n\tbuffer_load_dwordx4 %1, %2, %3 offen sc1 lds\n\tbuffer_load_dwordx4 %1, %2, %3 offen offset:1024 sc1 lds\n\t"
                     "buffer_load_dwordx4 %1, %2, %3 offen offset:2048 sc1 lds\n\tbuffer_load_dwordx4 %1, %2, %3 offen offset:3072 sc1 lds" ::"s"(lds), "v"(v), "s"(r), "s"(s) : "memory");
        dma_run<N - 4>(r, lds + 4096, v, s + 4096);
    } else if constexpr (N > 0) {
        dma_c(r, lds, v, s);
        dma_run<N - 1>(r, lds + 1024, v, s + 1024);
    }
}
DI void warm(rsrc_t r, unsigned lds, unsigned v, unsigned s) {
    asm volatile("s_mov_b32 m0, %0\n\ts_nop 0\n\tbuffer_load_dword %1, %2, %3 offen sc1 lds" ::"s"(lds), "v"(v), "s"(r), "s"(s) : "memory");
}
template <int N> DI void wait_vm() { asm volatile("s_waitcnt vmcnt(%0)" ::"n"(N) : "memory"); }
DI void wait_lds() { asm volatile("s_waitcnt lgkmcnt(0)" ::: "memory"); }
DI void bar() { asm volatile("s_barrier" ::: "memory"); }
DI void bar_lds() { asm volatile("s_waitcnt lgkmcnt(0)\n\ts_barrier" ::: "memory"); }
template <int N, int EXTRA> DI void drain_steps() { wait_vm<6 * N + EXTRA>(); bar(); if constexpr (N > 0) drain_steps<N - 1, EXTRA>(); }

template <int N> struct ic { static constexpr int value = N; };
template <class T> DI LAS T* lp(int off) { return (LAS T*)(size_t)(unsigned)off; }
DI v4u lds128(int off) { return *lp<v4u>(off); }
DI void lds_st128(int off, v4u d) { *lp<v4u>(off) = d; }
DI void lds_st64(int off, v2u d) { *lp<v2u>(off) = d; }
DI v2u lds_tr(int off) { return __builtin_bit_cast(v2u, __builtin_amdgcn_ds_read_tr16_b64_v4i16(lp<s16x4>(off))); }
DI v4u lds_tr2(int a0, int a1) { const v2u x0 = lds_tr(a0), x1 = lds_tr(a1); return v4u{x0.x, x0.y, x1.x, x1.y}; }

DI f32x16 mfma32(v4u a, v4u b, f32x16 c) { return __builtin_amdgcn_mfma_f32_32x32x16_bf16(__builtin_bit_cast(bf16x8, a), __builtin_bit_cast(bf16x8, b), c, 0, 0, 0); }
DI f32x4 mfma16(v4u a, v4u b, f32x4 c) { return __builtin_amdgcn_mfma_f32_16x16x32_bf16(__builtin_bit_cast(bf16x8, a), __builtin_bit_cast(bf16x8, b), c, 0, 0, 0); }
template <bool FIRST> DI void mma16(f32x4& c, v4u a, v4u b) {
    if constexpr (FIRST) asm volatile("v_mfma_f32_16x16x32_bf16 %0, %1, %2, 0" : "=&v"(c) : "v"(a), "v"(b) : "memory");
    else asm volatile("v_mfma_f32_16x16x32_bf16 %0, %1, %2, %0" : "+v"(c) : "v"(a), "v"(b) : "memory");
}
template <bool FIRST> DI void mma32(f32x16& c, v4u a, v4u b) {
    if constexpr (FIRST) asm volatile("v_mfma_f32_32x32x16_bf16 %0, %1, %2, 0" : "=&v"(c) : "v"(a), "v"(b) : "memory");
    else asm volatile("v_mfma_f32_32x32x16_bf16 %0, %1, %2, %0" : "+v"(c) : "v"(a), "v"(b) : "memory");
}
DI void mma16f(bool first, f32x4& c, v4u a, v4u b) { if (first) mma16<true>(c, a, b); else mma16<false>(c, a, b); }
DI void mma32f(bool first, f32x16& c, v4u a, v4u b) { if (first) mma32<true>(c, a, b); else mma32<false>(c, a, b); }
DI void mma_end(f32x16& a) { asm volatile("s_nop 7\n\ts_nop 4" : "+v"(a)); }
DI void mma_end(f32x16& a, f32x16& b, f32x16& c) { asm volatile("s_nop 7\n\ts_nop 4" : "+v"(a), "+v"(b), "+v"(c)); }
DI void mma_end(f32x4 (&a)[5]) { asm volatile("s_nop 7\n\ts_nop 0" : "+v"(a[0]), "+v"(a[1]), "+v"(a[2]), "+v"(a[3]), "+v"(a[4])); }
DI void mma_end(f32x4 (&a)[7]) { asm volatile("s_nop 7\n\ts_nop 0" : "+v"(a[0]), "+v"(a[1]), "+v"(a[2]), "+v"(a[3]), "+v"(a[4]), "+v"(a[5]), "+v"(a[6])); }
struct Frag {
    unsigned b0, b1, b2;
    DI explicit Frag(unsigned l16) : b0(l16), b1(l16 + 65536), b2(l16 + 131072) { asm volatile("" : "+v"(b0), "+v"(b1), "+v"(b2)); }
    DI v4u operator()(int off) const { return off < 65536 ? lds128(b0 + off) : off < 131072 ? lds128(b1 + (off - 65536)) : lds128(b2 + (off - 131072)); }
};
DI unsigned pk2v(f32x2 p) { bf16x2 q = __builtin_convertvector(p, bf16x2); return __builtin_bit_cast(unsigned, q); }
DI unsigned pk2(float lo, float hi) { return pk2v(f32x2{lo, hi}); }
DI float bflo(unsigned u) { return __uint_as_float(u << 16); }
DI float bfhi(unsigned u) { return __uint_as_float(u & 0xffff0000u); }
DI float dot2(unsigned a, unsigned b, float c) { return __builtin_amdgcn_fdot2_f32_bf16(__builtin_bit_cast(bf16x2, a), __builtin_bit_cast(bf16x2, b), c, false); }
DI float sum8(v4u v, float a) { unsigned x0 = v.x, x1 = v.y, x2 = v.z, x3 = v.w; return dot2(x0, 0x3f803f80u, dot2(x1, 0x3f803f80u, dot2(x2, 0x3f803f80u, dot2(x3, 0x3f803f80u, a)))); }
DI float sum_x16(float v) { unsigned u = __float_as_uint(v); auto r = __builtin_amdgcn_permlane16_swap(u, u, false, false); unsigned a = r[0], b = r[1]; return __uint_as_float(a) + __uint_as_float(b); }
DI float sum_x32(float v) { unsigned u = __float_as_uint(v); auto r = __builtin_amdgcn_permlane32_swap(u, u, false, false); unsigned a = r[0], b = r[1]; return __uint_as_float(a) + __uint_as_float(b); }
DI void swap16(unsigned& a, unsigned& b) { auto r = __builtin_amdgcn_permlane16_swap(a, b, false, false); a = r[0]; b = r[1]; }
DI void swap32(unsigned& a, unsigned& b) { auto r = __builtin_amdgcn_permlane32_swap(a, b, false, false); a = r[0]; b = r[1]; }
DI void swap32f(float& a, float& b) { unsigned x = __float_as_uint(a), y = __float_as_uint(b); swap32(x, y); a = __uint_as_float(x); b = __uint_as_float(y); }
DI f32x2 exp2x2(f32x2 v) { return f32x2{__builtin_amdgcn_exp2f(v.x), __builtin_amdgcn_exp2f(v.y)}; }
DI f32x2 fma2(f32x2 a, f32x2 b, f32x2 c) { return __builtin_elementwise_fma(a, b, c); }
DI f32x2 sp2(float v) { return f32x2{v, v}; }

DI void set_flag(char* ws, unsigned off, u64 tag) { __hip_atomic_store((GAS u64*)(ws + off), tag, __ATOMIC_RELAXED, __HIP_MEMORY_SCOPE_AGENT); }
DI void arrive4(int cnt, int lane, char* ws, unsigned off, u64 tag) {
    if (lane == 0) {
        const unsigned old = __hip_atomic_fetch_add(lp<unsigned>(cnt), 1u, __ATOMIC_RELAXED, __HIP_MEMORY_SCOPE_WORKGROUP);
        if ((old & 3u) == 3u) set_flag(ws, off, tag);
    }
}
DI void lds_wait(int off, u64 tag) {
    for (int sp = 0; sp < (1 << 22); ++sp)
        if (__all(*lp<volatile u64>(off) == tag)) break; else __builtin_amdgcn_s_sleep(1);
    asm volatile("" ::: "memory");
}
DI void poll(char* ws, unsigned off, u64 tag) {
    GAS u64* p = (GAS u64*)(ws + off);
    for (int sp = 0; sp < (1 << 17); ++sp) {
        if (__hip_atomic_load(p, __ATOMIC_RELAXED, __HIP_MEMORY_SCOPE_AGENT) == tag) break;
        if (POLL_SLEEP) __builtin_amdgcn_s_sleep(POLL_SLEEP);
    }
    __builtin_amdgcn_fence(__ATOMIC_ACQUIRE, "wavefront");
    asm volatile("" ::: "memory");
}
DI void lds_wait_ge(int off, unsigned n) {
    for (int sp = 0; sp < (1 << 22); ++sp)
        if (__all(*lp<volatile unsigned>(off) >= n)) break; else __builtin_amdgcn_s_sleep(1);
    asm volatile("" ::: "memory");
}

DI void feature_map(const f32x2 (&z)[10], float rs, unsigned (&o)[20]) {
    float mx = fmaxf(z[0].x, z[0].y), mn = fminf(z[0].x, z[0].y);
#pragma unroll
    for (int i = 1; i < 10; ++i) { mx = fmaxf(fmaxf(mx, z[i].x), z[i].y); mn = fminf(fminf(mn, z[i].x), z[i].y); }
    f32x2 ep[10], en[10];
#pragma unroll
    for (int i = 0; i < 10; ++i) { ep[i] = exp2x2(fma2(z[i], sp2(rs), sp2(-rs * mx))); en[i] = exp2x2(fma2(z[i], sp2(-rs), sp2(rs * mn))); }
    f32x2 sp = ep[0], sn = en[0];
#pragma unroll
    for (int i = 1; i < 10; ++i) { sp += ep[i]; sn += en[i]; }
    const float rp = __builtin_amdgcn_rcpf(sp.x + sp.y), rn = __builtin_amdgcn_rcpf(sn.x + sn.y);
#pragma unroll
    for (int i = 0; i < 10; ++i) { o[i] = pk2v(ep[i] * rp); o[10 + i] = pk2v(en[i] * rn); }
}

DI f32x2 geglu2(f32x2 G, f32x2 U, float K1, float K2, float IV) {
    const f32x2 e = exp2x2(G * fma2(G * G, sp2(K2), sp2(K1)));
    const f32x2 d = fma2(e, sp2(IV), sp2(IV));
    return G * U * f32x2{__builtin_amdgcn_rcpf(d.x), __builtin_amdgcn_rcpf(d.y)};
}
DI void p3_epilogue(const f32x4 (&acc)[2][7], const f32x4 (&cs)[7], const v2u (&st)[2][2], unsigned (&hh)[2][7]) {
    float c6[4] = {cs[6].x, cs[6].y, cs[6].z, cs[6].w};
    swap32f(c6[0], c6[2]); swap32f(c6[1], c6[3]);
#pragma unroll
    for (int k = 0; k < 2; ++k) {
        const float s = sum_x32(sum_x16(__uint_as_float(st[k][0].x) + __uint_as_float(st[k][1].x)));
        const float q = sum_x32(sum_x16(__uint_as_float(st[k][0].y) + __uint_as_float(st[k][1].y)));
        const float nm = s * (-1.f / 640.f), IV = fmaxf(q * (1.f / 640.f) - nm * nm, 0.f) + 1e-5f;
        const float rs = __builtin_amdgcn_rsqf(IV);
        const float K1 = (-2.f * 0.7978845608028654f * LOG2E) * rs, K2 = (0.044715f * K1) * (rs * rs);
        float z6[4] = {acc[k][6].x, acc[k][6].y, acc[k][6].z, acc[k][6].w};
        swap32f(z6[0], z6[2]); swap32f(z6[1], z6[3]);
#pragma unroll
        for (int u = 0; u < 3; ++u) {
            hh[k][2 * u] = pk2v(geglu2(fma2(cs[u].xy, sp2(nm), acc[k][u].xy), fma2(cs[u + 3].xy, sp2(nm), acc[k][u + 3].xy), K1, K2, IV));
            hh[k][2 * u + 1] = pk2v(geglu2(fma2(cs[u].zw, sp2(nm), acc[k][u].zw), fma2(cs[u + 3].zw, sp2(nm), acc[k][u + 3].zw), K1, K2, IV));
        }
        hh[k][6] = pk2v(geglu2(fma2(f32x2{c6[0], c6[1]}, sp2(nm), f32x2{z6[0], z6[1]}), fma2(f32x2{c6[2], c6[3]}, sp2(nm), f32x2{z6[2], z6[3]}), K1, K2, IV));
    }
}

#define STAMP(i) do { if ((i) == AMP_A) ampa = __builtin_amdgcn_s_memtime(); if ((i) == AMP_B) ampb = __builtin_amdgcn_s_memtime(); } while (0)
#define CSTAMP(i) do {} while (0)
#define SB() __builtin_amdgcn_sched_barrier(0)

__global__ __launch_bounds__(512, 2) void fused_block(const u16* __restrict__ x, const u16* __restrict__ wqkv, const u16* __restrict__ wphi,
                                                      const u16* __restrict__ wo, const u16* __restrict__ wg, const u16* __restrict__ wu,
                                                      const u16* __restrict__ wd, u16* __restrict__ out, char* __restrict__ ws) {
    __shared__ __attribute__((aligned(16))) char smem[LDS_BYTES];
    asm volatile("" ::"v"((LAS char*)smem));
    u64 ampa = 0, ampb = 0;
    STAMP(0); CSTAMP(62);
    asm volatile("" ::"s"(x), "s"(wqkv), "s"(wphi), "s"(wo), "s"(wg), "s"(wu), "s"(wd), "s"(out), "s"(ws));
    const int tid = threadIdx.x, lane = tid & 63;
    const int wv = __builtin_amdgcn_readfirstlane(tid >> 6);
    const int b = blockIdx.x, xcd = b & 7, rk = b >> 3;
    u64 pc; unsigned clen;
    asm volatile("s_getpc_b64 %0\nfb_code_lo:\n\ts_mov_b32 %1, fb_code_hi-fb_code_lo" : "=s"(pc), "=s"(clen));
    auto codepf = [&]() {
        const rsrc_t rc = mk_rsrc((const void*)pc, clen);
        const unsigned v = __builtin_amdgcn_raw_buffer_load_b32(rc, lane < 20 ? (lane * 32 + rk) * 128 : OOB, 0, 0);
        asm volatile("" ::"v"(v));
    };
    if (wv == 3) {
        const rsrc_t rc = mk_rsrc((const void*)pc, clen);
        if (lane < 20) asm volatile("s_mov_b32 m0, %0\n\ts_nop 0\n\tbuffer_load_dword %1, %2, 0 offen lds" ::"s"(L_PF), "v"((lane * 32 + rk) * 128), "s"(rc) : "memory");
        if (lane < 16) *lp<unsigned>(L_CNT + lane * 4) = 0u;
    }
    const int m = (xcd >> 1) * 4 + (rk >> 3), hp = (xcd & 1) * 8 + (rk & 7);
    const int t = (xcd >> 1) * 8 + (rk >> 2), c = (xcd & 1) * 4 + (rk & 3);
    const int TT = (xcd >> 2) * 4 + (rk >> 3), j = (xcd & 3) * 8 + (rk & 7);
    const rsrc_t rws = mk_rsrc(ws, WS_USED);
    const int l15 = lane & 15, lg = lane >> 4, l31 = lane & 31, lh = lane >> 5;
    const unsigned l16 = lane * 16;
    const v2u ep = ld64c(rws, OFF_EPOCH, 0);
    u64 tag;
    auto gather = [&]() {
        v4u g[8];
        const unsigned go = (32 * wv + l31) * 16 + lh * 8 * 3584;
#pragma unroll
        for (int i = 0; i < 8; ++i) g[i] = ld128c(rws, go, OFF_KVP + (hp * 16 + ((i + 2 * m) & 7)) * 3584);
        SB();
        f32x2 v[4] = {};
#pragma unroll
        for (int i = 0; i < 8; ++i)
#pragma unroll
            for (int e = 0; e < 4; ++e) { const unsigned u = g[i][e]; v[e] += f32x2{bflo(u), bfhi(u)}; }
        float z[8] = {v[0].x, v[0].y, v[1].x, v[1].y, v[2].x, v[2].y, v[3].x, v[3].y};
#pragma unroll
        for (int e = 0; e < 4; ++e) { swap32f(z[e], z[e + 4]); z[e] += z[e + 4]; }
        lds_st64(L_KVT + (32 * wv + l31) * 16 + lh * 8, v2u{pk2(z[0], z[1]), pk2(z[2], z[3])});
    };
    if (wv < 4) {
        unsigned phq[20];
        v4u xr0, xr1; v2u xr2;
        {
        v4u a[40];
        float mean1, rs1;
        {
            const rsrc_t rx = mk_rsrc(x, SEQ * DM * 2);
            const int lx = L_X + wv * 32768;
            unsigned vo[2];
#pragma unroll
            for (int g = 0; g < 2; ++g) vo[g] = (lane >> 3) * 1280 + (((lane & 7) ^ ((4 * g + (lane >> 4)) & 7)) * 16);
            const unsigned so = (128 * m + 32 * wv) * 1280;
            auto blk = [&](int bb) {
#pragma unroll
                for (int g = 0; g < 4; ++g) dma_p(rx, lx + (bb % 8) * 4096 + g * 1024, vo[g & 1], so + g * 10240 + bb * 128);
            };
#pragma unroll
            for (int bb = 0; bb < 8; ++bb) blk(bb);
            STAMP(20);
            unsigned ra[4];
#pragma unroll
            for (int s = 0; s < 4; ++s) ra[s] = lx + l31 * 128 + (((2 * s + lh) ^ ((l31 >> 1) & 7)) * 16);
            float s0 = 0.f, s1 = 0.f, q0 = 0.f, q1 = 0.f;
            auto rd = [&](int bb) {
#pragma unroll
                for (int s = 0; s < 4; ++s) a[4 * bb + s] = lds128(ra[s] + (bb % 8) * 4096);
            };
            auto stat = [&](int bb) {
#pragma unroll
                for (int s = 0; s < 4; ++s) {
                    const unsigned e0 = a[4 * bb + s].x, e1 = a[4 * bb + s].y, e2 = a[4 * bb + s].z, e3 = a[4 * bb + s].w;
                    s0 = dot2(e0, 0x3f803f80u, s0); q0 = dot2(e0, e0, q0); s1 = dot2(e1, 0x3f803f80u, s1); q1 = dot2(e1, e1, q1);
                    s0 = dot2(e2, 0x3f803f80u, s0); q0 = dot2(e2, e2, q0); s1 = dot2(e3, 0x3f803f80u, s1); q1 = dot2(e3, e3, q1);
                }
                SB();
            };
            wait_vm<24>(); STAMP(21); rd(0); rd(1); wait_lds(); blk(8); blk(9);
            STAMP(45);
            wait_vm<24>(); rd(2); rd(3); SB(); stat(0); stat(1); stat(2); stat(3);
            wait_vm<16>(); rd(4); rd(5); SB(); stat(4); stat(5);
            wait_vm<8>(); rd(6); rd(7); SB(); stat(6); stat(7);
            wait_vm<0>(); rd(8); rd(9); SB(); stat(8); stat(9);
            const float s = sum_x32(s0 + s1), q = sum_x32(q0 + q1);
            mean1 = s * (1.f / 640.f);
            rs1 = __builtin_amdgcn_rsqf(fmaxf(q * (1.f / 640.f) - mean1 * mean1, 0.f) + 1e-5f);
        }
        tag = (((u64)__builtin_amdgcn_readfirstlane(ep.y) << 32) | __builtin_amdgcn_readfirstlane(ep.x)) + 1;
        STAMP(1);
        bar_lds();
        STAMP(3);
        f32x16 acc[4];
        const Frag frag(l16);
        bar();
        STAMP(40); CSTAMP(50);
        v4u w[6];
#pragma unroll
        for (int u = 0; u < 6; ++u) w[u] = frag(u * 1024);
        SB();
#pragma unroll
        for (int i = 0; i < 40; ++i) {
            if (i == 6 || i == 12 || i == 26) bar();
#pragma unroll
            for (int u = 0; u < 3; ++u) {
                mma32f(i == 0, acc[u], w[(i & 1) * 3 + u], a[i]);
                if (i + 2 < 40) w[(i & 1) * 3 + u] = frag(((i + 2) * 3 + u) * 1024);
            }
        }
        mma_end(acc[0], acc[1], acc[2]);
        STAMP(4); CSTAMP(51);
        lds_wait(L_TG + 32, tag);
        auto centred = [&](int idx0, f32x2 (&z)[10]) {
#pragma unroll
            for (int i = 0; i < 5; ++i) {
                const f32x4 cs = *lp<f32x4>(L_C1 + lh * 320 + (idx0 + 4 * i) * 4);
                const int e = idx0 + 4 * i;
                z[2 * i] = fma2(cs.xy, sp2(-mean1), f32x2{acc[e >> 4][e & 15], acc[e >> 4][(e & 15) + 1]});
                z[2 * i + 1] = fma2(cs.zw, sp2(-mean1), f32x2{acc[e >> 4][(e & 15) + 2], acc[e >> 4][(e & 15) + 3]});
            }
        };
        {
            f32x2 z[10]; unsigned pk[20];
            const int ro = L_PKV + (lh * 128 + 32 * wv + l31) * 144;
            centred(20, z);
#pragma unroll
            for (int i = 0; i < 10; ++i) pk[i] = pk2v(z[i] * rs1);
            lds_st128(ro + 80, v4u{pk[0], pk[1], pk[2], pk[3]});
            lds_st128(ro + 96, v4u{pk[4], pk[5], pk[6], pk[7]});
            lds_st128(ro + 112, v4u{pk[8], pk[9], 0x3f80u, 0u});
            centred(0, z);
            feature_map(z, rs1, pk);
#pragma unroll
            for (int i = 0; i < 5; ++i) lds_st128(ro + i * 16, v4u{pk[4 * i], pk[4 * i + 1], pk[4 * i + 2], pk[4 * i + 3]});
        }
        STAMP(22); CSTAMP(57);
        bar_lds();
        {
            const int head = wv >> 1, ft = wv & 1, blk = lg & 1, q = l15 >> 2, p = lane & 3;
            const int rb = L_PKV + (head * 128 + 8 * lh + q) * 144;
            const int ao = rb + (32 * ft + 16 * blk + 8 * (p & 1) + 4 * (p >> 1)) * 2, bo = rb + (40 + 16 * blk + 4 * p) * 2;
            f32x16 d = {};
            v4u ta[4], tb[4];
#pragma unroll
            for (int s = 0; s < 4; ++s) { ta[s] = lds_tr2(ao + s * 2304, ao + s * 2304 + 576); tb[s] = lds_tr2(bo + s * 2304, bo + s * 2304 + 576); }
            SB();
#pragma unroll
            for (int s = 0; s < 8; ++s) {
                d = mfma32(ta[s & 3], tb[s & 3], d);
                if (s < 4) { ta[s] = lds_tr2(ao + (s + 4) * 2304, ao + (s + 4) * 2304 + 576); tb[s] = lds_tr2(bo + (s + 4) * 2304, bo + (s + 4) * 2304 + 576); }
                SB();
            }
            const v4u c0 = {pk2(d[0], d[1]), pk2(d[2], d[3]), pk2(d[4], d[5]), pk2(d[6], d[7])};
            const v4u c1 = {pk2(d[8], d[9]), pk2(d[10], d[11]), pk2(d[12], d[13]), pk2(d[14], d[15])};
            const unsigned o = OFF_KVP + (hp * 16 + m) * 3584 + ((head * 5 + 4 * ft + lh) * 21 + l31) * 16;
            st128c(c0, rws, (l31 < 21 && (ft == 0 || lh == 0)) ? o : OOB, 0);
            st128c(c1, rws, (l31 < 21 && ft == 0) ? o + 2 * 21 * 16 : OOB, 0);
        }
        STAMP(23);
        unsigned lq = L_W1Q + (lh * 24 + (l31 < 24 ? l31 : l31 - 8)) * 16;
        asm volatile("" : "+v"(lq));
#pragma unroll
        for (int u = 0; u < 6; ++u) w[u] = lds128(lq + u * 768);
        SB();
#pragma unroll
        for (int i = 0; i < 40; ++i) {
            if (i == P1_FLAG_AT) { wait_vm<0>(); arrive4(L_CNT, lane, ws, OFF_FA + (hp * 16 + m) * 128, tag); STAMP(5); }
            mma32f(i == 0, acc[3], w[i % 6], a[i]);
            if (i + 6 < 40) w[i % 6] = lds128(lq + (i + 6) * 768);
        }
        mma_end(acc[3]);
        STAMP(24);
        {
            const rsrc_t rx = mk_rsrc(x, SEQ * DM * 2);
            const unsigned o = ((64 * t + 16 * wv + l15) * 640 + 80 * c + 20 * lg) * 2;
            xr0 = ld128p(rx, o, 0); xr1 = ld128p(rx, o + 16, 0); xr2 = ld64p(rx, o + 32, 0);
        }
        f32x2 z[10];
        centred(40, z);
        feature_map(z, rs1, phq);
#pragma unroll
        for (int i = 0; i < 20; ++i) asm volatile("" : "+v"(phq[i]));
        STAMP(6);
        }
        bar();
        STAMP(7);
        gather();
        bar_lds();
        STAMP(8);
        unsigned hk[10];
        {
            const int i16 = (l31 & 3) + 4 * (l31 >> 3);
            const bool mine = ((l31 >> 2) & 1) == lh;
            const int a0 = mine ? L_KVT + (lh * 105 + i16) * 16 : L_ZR, a1 = mine ? L_KVT + (lh * 105 + 16 + (i16 < 5 ? i16 : 4)) * 16 : L_ZR;
            v4u k0[5], k1[5];
#pragma unroll
            for (int s = 0; s < 5; ++s) { k1[s] = lds128(a1 + s * 336); k0[s] = lds128(a0 + s * 336); }
            SB();
            f32x16 d0 = {}, d1 = {};
#pragma unroll
            for (int s = 0; s < 5; ++s) {
                const v4u bq = {phq[4 * s], phq[4 * s + 1], phq[4 * s + 2], phq[4 * s + 3]};
                d1 = mfma32(k1[s], bq, d1);
                d0 = mfma32(k0[s], bq, d0);
            }
            const float rd = __builtin_amdgcn_rcpf(d1[4] + 1e-6f);
            const v4u c0 = {pk2(d0[0] * rd, d0[1] * rd), pk2(d0[2] * rd, d0[3] * rd), pk2(d0[4] * rd, d0[5] * rd), pk2(d0[6] * rd, d0[7] * rd)};
            const v4u c1 = {pk2(d0[8] * rd, d0[9] * rd), pk2(d0[10] * rd, d0[11] * rd), pk2(d0[12] * rd, d0[13] * rd), pk2(d0[14] * rd, d0[15] * rd)};
            const v2u c2 = {pk2(d1[0] * rd, d1[1] * rd), pk2(d1[2] * rd, d1[3] * rd)};
            const unsigned o = OFF_O + (8 * m + 2 * wv + (l31 >> 4)) * 20480 + 5 * hp * 256 + l15 * 16;
            st128c(c0, rws, o + lh * 512, 0);
            st128c(c1, rws, o + lh * 512 + 256, 0);
            st64c(c2, rws, o + 1024 + lh * 8, 0);
        }
        STAMP(25);
        {
        const Frag frag(l16);
        v4u w[30];
#pragma unroll
        for (int f = 0; f < 30; ++f) w[f] = frag(w2frag(f));
        wait_vm<0>();
        arrive4(L_CNT, lane, ws, OFF_FB + (m * 16 + hp) * 128, tag);
        STAMP(9);
        bar();
        STAMP(10);
        v4u a[20];
        const unsigned oo = OFF_O + (4 * t + wv) * 20480;
        auto old = [&](int i) { a[i] = ld128c(rws, l16, oo + i * 1024); };
        constexpr int PD = P2_PD;
#pragma unroll
        for (int i = 0; i < PD; ++i) old(i);
        SB();
        f32x4 acc[5];
#pragma unroll
        for (int f = 0; f < 100; ++f) {
            if (f % 5 == 0 && f / 5 + PD < 20) old(f / 5 + PD);
            mma16f(f < 5, acc[f % 5], w[f % 30], a[f / 5]);
            if (f + 30 < 100) w[f % 30] = frag(w2frag(f + 30));
        }
        *lp<volatile unsigned>(L_W2D + wv * 4) = 1u;
        mma_end(acc);
        STAMP(26);
        const unsigned xr[10] = {xr0.x, xr0.y, xr0.z, xr0.w, xr1.x, xr1.y, xr1.z, xr1.w, xr2.x, xr2.y};
        float s = 0.f, q = 0.f;
#pragma unroll
        for (int i = 0; i < 10; ++i) {
            hk[i] = pk2(acc[i >> 1][2 * (i & 1)] + bflo(xr[i]), acc[i >> 1][2 * (i & 1) + 1] + bfhi(xr[i]));
            s = dot2(hk[i], 0x3f803f80u, s); q = dot2(hk[i], hk[i], q);
        }
        const unsigned o = OFF_HB + (4 * t + wv) * 20480 + 10 * c * 256 + l15 * 16;
        st128c(v4u{hk[0], hk[1], hk[2], hk[3]}, rws, o + lg * 512, 0);
        st128c(v4u{hk[4], hk[5], hk[6], hk[7]}, rws, o + lg * 512 + 256, 0);
        st64c(v2u{hk[8], hk[9]}, rws, o + (8 + (lg >> 1)) * 256 + (lg & 1) * 8, 0);
        s = sum_x32(sum_x16(s)); q = sum_x32(sum_x16(q));
        st64c(v2u{__float_as_uint(s), __float_as_uint(q)}, rws, lane < 16 ? OFF_ST + ((4 * t + wv) * 8 + c) * 128 + lane * 8 : OOB, 0);
        STAMP(27);
        wait_vm<0>();
        arrive4(L_CNT, lane, ws, OFF_FC + (t * 8 + c) * 128, tag);
        STAMP(11);
        }
        bar();
        STAMP(12); CSTAMP(52);
        if (j < 31) {
            const int tt0 = 16 * TT + 4 * wv;
            const unsigned ho = OFF_HB + tt0 * 20480;
            const Frag frag(l16);
            constexpr int PD = P3_PD;
            v4u a[80], w[7];
            v2u st[2][2][2];
            unsigned rdy = 0;
            auto hld = [&](int f) { if (f < 80) a[f] = ld128c(rws, l16, ho + ((f / 40) * 2 + (f & 1)) * 20480 + ((f % 40) >> 1) * 1024); };
            auto stld = [&](int e) { st[e >> 2][(e >> 1) & 1][e & 1] = ld64c(rws, (2 * lg + (e & 1)) * 128 + l15 * 8, OFF_ST + (tt0 + (e >> 1)) * 1024); };
#pragma unroll
            for (int f = 0; f < 2 * PD; ++f) hld(f);
#pragma unroll
            for (int u = 0; u < 7; ++u) w[u] = frag(w3slot(0) + u * 1024);
#pragma unroll
            for (int ps = 0; ps < 2; ++ps) {
                f32x4 acc[2][7];
#pragma unroll
                for (int i = 0; i < 20; ++i) {
                    if (ps == 0 && (i == 11 || i == 15)) {
                        const unsigned need = i == 11 ? 2u : 3u;
                        if (!__all(rdy >= need)) {
                            for (int sp = 0; sp < (1 << 22); ++sp)
                                if (__all(*lp<volatile unsigned>(L_W3R + (lane & 3) * 4) >= need)) break;
                        }
                        asm volatile("" ::: "memory");
                    }
                    if (ps == 1 && i == 12) *lp<volatile unsigned>(L_PRG + wv * 4) = 1u;
                    if (ps == 1 && i == P3_FLAG_AT) {
                        wait_vm<2 * P3_FLAG_AT>();
                        if (lane == 0) set_flag(ws, OFF_FD + (((4 * TT + wv) * 2) * 32 + j) * 128, tag);
                    }
#pragma unroll
                    for (int u = 0; u < 7; ++u) {
                        mma16f(i == 0, acc[0][u], w[u], a[40 * ps + 2 * i]);
                        if (u == 2 || u == 5) hld(40 * ps + 2 * (i + PD) + (u == 5));
                        if (u == 0 && i >= 8 && i < 12) stld(4 * ps + i - 8);
                        if (ps == 0 && u == 3 && (i == 10 || i == 14)) rdy = *lp<volatile unsigned>(L_W3R + (lane & 3) * 4);
                        mma16f(i == 0, acc[1][u], w[u], a[40 * ps + 2 * i + 1]);
                        if (i + 1 < 20 || ps == 0) w[u] = frag(w3slot((i + 1) % 20) + u * 1024);
                    }
                }
                mma_end(acc[0]); mma_end(acc[1]);
                if (ps == 0) { STAMP(32); CSTAMP(53); } else { STAMP(13); CSTAMP(58); }
                f32x4 cs[7];
#pragma unroll
                for (int u = 0; u < 7; ++u) cs[u] = *lp<f32x4>(L_CS + (u * 16 + lg * 4) * 4);
                if (ps == 1) bar_lds();
                unsigned hh[2][7];
                p3_epilogue(acc, cs, st[ps], hh);
                const unsigned o = OFF_HH + (tt0 + 2 * ps) * 55296 + 7 * j * 256 + l15 * 16;
                st128c(v4u{hh[0][0], hh[0][1], hh[0][2], hh[0][3]}, rws, o + lg * 256, 0);
                st128c(v4u{hh[1][0], hh[1][1], hh[1][2], hh[1][3]}, rws, o + lg * 256, 55296);
                swap16(hh[0][4], hh[1][4]); swap16(hh[0][5], hh[1][5]); swap16(hh[0][6], hh[1][6]);
                st128c(v4u{hh[0][4], hh[0][5], hh[1][4], hh[1][5]}, rws, o + (lg & 1) * 55296 + (4 + (lg >> 1)) * 256, 0);
                st64c(v2u{hh[0][6], hh[1][6]}, rws, j < 30 ? o + (lg & 1) * 55296 + 6 * 256 + (lg >> 1) * 8 : OOB, 0);
                asm volatile("" ::: "memory");
                if (ps == 0) { STAMP(33); CSTAMP(54); } else { STAMP(29); CSTAMP(59); }
            }
            bar();
            wait_vm<0>();
            if (lane == 0) set_flag(ws, OFF_FD + (((4 * TT + wv) * 2 + 1) * 32 + j) * 128, tag);
            STAMP(14);
        } else {
            bar();
            bar();
        }
        if (wv == 0) {
            __builtin_amdgcn_s_sleep(D_SLEEP);
            poll(ws, OFF_FD + ((t * 2 + 1) * 32 + (l31 < 31 ? l31 : 30)) * 128, tag);
        }
        bar();
        bar();
        asm volatile("fb_code_hi:");
    } else {
        {
            const int isq = (wv - 4) >> 1, fhead = wv & 1, lf = L_F + (wv - 4) * 5120;
            if (P0_PRIO) __builtin_amdgcn_s_setprio(P0_PRIO);
            const rsrc_t rq = mk_rsrc(wqkv, DM * 3 * DM * 2), rp = mk_rsrc(wphi, 32 * 400 * 2);
            const unsigned so = (40 * m * 1920 + (2 * hp + fhead) * 20) * 2;
            if (lane < 63) {
                const unsigned vo = (lane / 3) * 3840 + (lane % 3) * 16;
                dma_p(rq, lf, vo, so + (isq ? 0 : 1280)); dma_p(rq, lf + 1008, lane < 57 ? vo : OOB, so + (isq ? 0 : 1280) + 21 * 3840);
                dma_p(rp, lf + 2016, lane < 50 ? l16 : OOB, (2 * hp + fhead) * 800);
                dma_p(rq, lf + 3024, (isq && lane >= 48) ? OOB : vo, so + 2560 + (isq ? 24 * 3840 : 0));
                dma_p(rq, lf + 4032, (isq || lane >= 9) ? OOB : vo, so + 2560 + 21 * 3840);
            }
            const int r = (l31 & 3) + 4 * (l31 >> 3), hh = (l31 >> 2) & 1;
            const int a1 = lf + (16 * (r >> 3) + 8 * hh + (r & 7)) * 48, a2 = lf + (32 + (r & 7)) * 48;
            const int tc = (16 * (lg & 1) + 4 * (lane & 3)) * 2;
            const int pa = lf + 2016 + (l15 >> 2) * 40 + tc, va = lf + 3024 + (8 * lh + (l15 >> 2)) * 48 + tc;
            const unsigned ob = OFF_W1 + hp * 163840 + (5 * m + lh) * 2048 + fhead * 1024 + l31 * 16;
            const unsigned o = l31 < 20 ? ob + (isq ? 640 : 0) : OOB, ov = l31 < 20 ? ob + 320 + (isq ? 6144 : 0) : OOB;
            STAMP(20);
            wait_vm<0>();
            STAMP(21);
            const v4u fa0 = lds128(a1 + 16 * lh), fc0 = lds128(a2 + 16 * lh);
            const v2u fa1 = *lp<v2u>(a1 + 32), fc1 = *lp<v2u>(a2 + 32);
            const v4u b0 = lds_tr2(pa + lh * 320, pa + lh * 320 + 160);
            const v2u p2 = lds_tr(pa + 640);
            const v4u v0 = lds_tr2(va, va + 192), v1 = lds_tr2(va - lh * 384 + 768, va - lh * 384 + 960);
            const v4u b1 = {lh == 0 ? p2.x : 0u, lh == 0 ? p2.y : 0u, 0u, 0u};
            f32x16 d = {}, e = {};
            d = mfma32(fa0, b0, d);
            e = mfma32(fc0, b0, e);
            d = mfma32(v4u{fa1.x, fa1.y, 0u, 0u}, b1, d);
            e = mfma32(v4u{fc1.x, fc1.y, 0u, 0u}, b1, e);
            const bool third = lh == 0 && !isq;
            st128c(v0, rws, ov, 0);
            st128c(v1, rws, third ? ov : OOB, 4096);
            const float sv = sum_x32(sum8(v0, third ? sum8(v1, 0.f) : 0.f));
            const float sc = LOG2E;
            const v4u c0 = {pk2(d[0] * sc, d[1] * sc), pk2(d[2] * sc, d[3] * sc), pk2(d[4] * sc, d[5] * sc), pk2(d[6] * sc, d[7] * sc)};
            const v4u c1 = {pk2(d[8] * sc, d[9] * sc), pk2(d[10] * sc, d[11] * sc), pk2(d[12] * sc, d[13] * sc), pk2(d[14] * sc, d[15] * sc)};
            const v4u c2 = {pk2(e[0] * sc, e[1] * sc), pk2(e[2] * sc, e[3] * sc), pk2(e[4] * sc, e[5] * sc), pk2(e[6] * sc, e[7] * sc)};
            st128c(c0, rws, o, 0);
            st128c(c1, rws, o, 4096);
            st128c(c2, rws, lh == 0 ? o : OOB, 8192);
            const float sw = sum_x32(sum8(c0, sum8(c1, lh == 0 ? sum8(c2, 0.f) : 0.f)));
            st32c(__float_as_uint(lh == 0 ? sw : sv), rws, l31 < 20 ? OFF_C1P + (hp * 16 + m) * 640 + fhead * 320 + (isq * 40 + lh * 20 + l31) * 4 : OOB, 0);
        }
        tag = (((u64)__builtin_amdgcn_readfirstlane(ep.y) << 32) | __builtin_amdgcn_readfirstlane(ep.x)) + 1;
        STAMP(1);
        wait_vm<0>();
        *lp<volatile u64>(L_TG + (wv - 4) * 8) = tag;
        if (wv == 4) {
            lds_wait(L_TG + (lane & 3) * 8, tag);
            if (lane == 0) set_flag(ws, OFF_F0 + (hp * 16 + m) * 128, tag);
            STAMP(2);
            __builtin_amdgcn_s_sleep(P0_DELAY);
            poll(ws, OFF_F0 + (hp * 16 + l15) * 128, tag);
        }
        if (P0_PRIO) __builtin_amdgcn_s_setprio(0);
        bar();
        STAMP(3);
        const unsigned wb1 = OFF_W1 + hp * 163840;
        if (wv < 6) {
            const int lw = wv - 4, i16 = (l31 & 3) + 4 * (l31 >> 3);
            const unsigned wl = lh * 2048 + ((l31 >> 2) & 1) * 1024 + i16 * 16;
            auto issue = [&](int ch) {
#pragma unroll
                for (int u = 0; u < 6; ++u) dma_c(rws, ((4 * ch + 2 * lw) * 3 + u) * 1024, wl, wb1 + (4 * ch + 2 * lw + u / 3) * 4096 + (u % 3) * 256);
            };
            issue(0); issue(1); issue(2); issue(3); wait_vm<12>(); bar();
            issue(4); issue(5); issue(6); wait_vm<18>(); bar();
            issue(7); issue(8); issue(9); wait_vm<18>(); bar();
            wait_vm<0>(); bar();
            {
                const int r3 = lane < 24 ? lane : lane - 24;
                const unsigned wl3 = (lane < 24 ? 0 : 2048) + ((r3 >> 2) & 1) * 1024 + (48 + (r3 & 3) + 4 * (r3 >> 3)) * 16;
                if (lane < 48) {
#pragma unroll
                    for (int i = 0; i < 20; ++i) dma_c(rws, L_W1Q + (2 * i + lw) * 768, wl3, wb1 + (2 * i + lw) * 4096);
                }
                wait_vm<0>();
            }
            STAMP(22);
            bar();
            __builtin_amdgcn_s_sleep(A_SLEEP);
            poll(ws, OFF_FWC + (c * 32 + l15) * 128, tag);
            STAMP(23);
            dma_run<40>(rws, lw * 77824, l16, OFF_W2 + c * 102400 + lw * 40960);
            dma_run<2>(rws, w2frag(80) + lw * 2048, l16, OFF_W2 + c * 102400 + 81920 + lw * 2048);
            STAMP(24);
        } else {
            bar();
            if (wv == 6) {
                v4u cp[16];
#pragma unroll
                for (int i = 0; i < 16; ++i) cp[i] = ld128c(rws, lane < 40 ? l16 : OOB, OFF_C1P + (hp * 16 + i) * 640);
                bar();
#pragma unroll
                for (int i = 0; i < 16; ++i) asm volatile("" : "+v"(cp[i]));
                bar();
                const rsrc_t rg = mk_rsrc(wg, DM * FF * 2), ru = mk_rsrc(wu, DM * FF * 2);
                const unsigned vo = (lane / 7) * (FF * 2) + (lane % 7) * 16, so = (80 * TT * FF + 56 * j) * 2;
                if (lane < 63) {
#pragma unroll
                    for (int i = 0; i < 9; ++i) { dma_p(rg, L_RG + i * 1008, vo, so + i * 9 * FF * 2); dma_p(ru, L_RU + i * 1008, vo, so + i * 9 * FF * 2); }
                }
                float c1s[4];
#pragma unroll
                for (int e = 0; e < 4; ++e) { const unsigned u = cp[0][e]; c1s[e] = __uint_as_float(u); }
#pragma unroll
                for (int i = 1; i < 16; ++i) {
                    const unsigned u0 = cp[i].x, u1 = cp[i].y, u2 = cp[i].z, u3 = cp[i].w;
                    asm("v_add_f32 %0, %0, %4\n\tv_add_f32 %1, %1, %5\n\tv_add_f32 %2, %2, %6\n\tv_add_f32 %3, %3, %7"
                        : "+v"(c1s[0]), "+v"(c1s[1]), "+v"(c1s[2]), "+v"(c1s[3]) : "v"(u0), "v"(u1), "v"(u2), "v"(u3));
                }
#pragma unroll
                for (int e = 0; e < 4; ++e) {
                    float o2 = __shfl(c1s[e], lane + 10);
                    o2 = ((lane >= 5 && lane < 10) || (lane >= 25 && lane < 30)) ? o2 : 0.f;
                    asm("v_add_f32 %0, %0, %1" : "+v"(c1s[e]) : "v"(o2));
                }
                if (lane < 40) *lp<f32x4>(L_C1 + l16) = f32x4{c1s[0], c1s[1], c1s[2], c1s[3]};
                wait_lds();
                *lp<volatile u64>(L_TG + 32) = tag;
            } else {
                bar(); bar();
                const rsrc_t rd = mk_rsrc(wd, DM * FF * 2), ro = mk_rsrc(wo, DM * DM * 2);
                const unsigned vo = (lane / 10) * (DM * 2) + (lane % 10) * 16;
                if (lane < 60) {
#pragma unroll
                    for (int i = 0; i < 10; ++i) dma_p(rd, L_RD + i * 960, vo, (56 * t * DM + 80 * c) * 2 + i * 6 * DM * 2);
#pragma unroll
                    for (int i = 0; i < 7; ++i) dma_p(ro, L_RO + i * 960, vo, (40 * t * DM + 80 * c) * 2 + i * 6 * DM * 2);
                }
            }
            STAMP(41);
            bar();
            const int q = l15 >> 2, p = lane & 3;
            v4u rt[18];
            unsigned dv[18];
            wait_vm<0>();
            STAMP(42);
            if (wv == 6) {
                const int ra = L_RG + (20 * lg + q) * 112, c6 = (p >> 1) * 9216 + (48 + 4 * (p & 1)) * 2;
                const int rb = L_RG + (40 * (lg & 1) + 16 + q) * 112;
#pragma unroll
                for (int hf = 0; hf < 2; ++hf) {
#pragma unroll
                    for (int ti = 0; ti < 6; ++ti) { const int a = ra + 24 * p + hf * 896 + (ti / 3) * 9216 + (ti % 3) * 8; rt[hf * 7 + ti] = lds_tr2(a, a + 448); }
                    rt[hf * 7 + 6] = lds_tr2(ra + c6 + hf * 896, ra + c6 + hf * 896 + 448);
                }
#pragma unroll
                for (int s = 0; s < 3; ++s) { const int a = rb + (lg >> 1) * 9216 + 24 * p + s * 8; rt[14 + s] = lds_tr2(a, a + 2240); }
                rt[17] = lds_tr2(rb + c6, rb + c6 + 2240);
                const bool on = j < 31;
                auto dst = [&](int Q) { return OFF_W3 + (j * 20 + (Q >> 2)) * 7168 + ((Q & 3) * 16 + l15) * 16; };
                const unsigned d0 = on ? dst(10 * TT + 2 * lg) : OOB, d1 = on ? dst(10 * TT + 2 * lg + 1) : OOB, d2 = on ? dst(10 * TT + 8 + (lg & 1)) : OOB;
#pragma unroll
                for (int ti = 0; ti < 7; ++ti) { dv[ti] = d0 + ti * 1024; dv[7 + ti] = d1 + ti * 1024; }
#pragma unroll
                for (int s = 0; s < 3; ++s) dv[14 + s] = d2 + (lg >> 1) * 3072 + s * 1024;
                dv[17] = lg < 2 ? d2 + 6144 : OOB;
            } else {
                const int ra = L_RD + (12 * lg + q) * 160 + 40 * p;
                const int r0 = lg < 2 ? 24 * lg + 8 + q : 48 + 4 * (q >> 1) + (q & 1), r1 = lg < 2 ? r0 + 12 : r0 + 2;
                const int oa = L_RO + (20 * (lg >> 1) + 8 * (lg & 1) + q) * 160 + 40 * p, ob = L_RO + (16 + q) * 160 + 40 * p;
                auto d4 = [&](int Q) { return Q < 216 ? OFF_W4 + (c * 54 + (Q >> 2)) * 5120 + ((Q & 3) * 16 + l15) * 16 : OOB; };
                auto d2 = [&](int Q) { return t < 16 ? OFF_W2 + (c * 20 + (Q >> 2)) * 5120 + ((Q & 3) * 16 + l15) * 16 : OOB; };
                const unsigned da = d4(7 * t + lg), db = lg < 3 ? d4(7 * t + 4 + lg) : OOB, dc = d2(5 * t + lg), dd = d2(5 * t + 4);
#pragma unroll
                for (int ti = 0; ti < 5; ++ti) {
                    rt[ti] = lds_tr2(ra + ti * 8, ra + ti * 8 + 640);
                    rt[5 + ti] = lds_tr2(L_RD + r0 * 160 + 40 * p + ti * 8, L_RD + r1 * 160 + 40 * p + ti * 8);
                    rt[10 + ti] = lds_tr2(oa + ti * 8, oa + ti * 8 + 640);
                    dv[ti] = da + ti * 1024; dv[5 + ti] = db + ti * 1024; dv[10 + ti] = dc + ti * 1024;
                }
                rt[15] = lds_tr2(ob + lg * 8, ob + lg * 8 + 3200);
                rt[16] = lds_tr2(ob + 32, ob + 32 + 3200);
                rt[17] = rt[16];
                dv[15] = dd + lg * 1024; dv[16] = lg == 0 ? dd + 4096 : OOB; dv[17] = OOB;
            }
            STAMP(43);
#pragma unroll
            for (int i = 17; i >= 0; --i) st128c(rt[i], rws, dv[i], 0);
            STAMP(44);
            bar();
            if (wv == 6) {
                float cs[8];
#pragma unroll
                for (int ti = 0; ti < 7; ++ti) cs[ti] = sum8(rt[ti], sum8(rt[7 + ti], 0.f));
#pragma unroll
                for (int s = 0; s < 3; ++s) { const float e = sum8(rt[14 + s], 0.f); cs[s] += lg < 2 ? e : 0.f; cs[s + 3] += lg < 2 ? 0.f : e; }
                { const float e = sum8(rt[17], 0.f); cs[6] += lg < 2 ? e : 0.f; }
#pragma unroll
                for (int ti = 0; ti < 7; ++ti) cs[ti] = sum_x32(sum_x16(cs[ti]));
                cs[7] = 0.f;
                const float ca = lg == 0 ? cs[0] : lg == 1 ? cs[1] : lg == 2 ? cs[2] : cs[3], cb = lg == 0 ? cs[4] : lg == 1 ? cs[5] : lg == 2 ? cs[6] : cs[7];
                st32c(__float_as_uint(ca), rws, lane * 4, OFF_CSP + (j * 8 + TT) * 512);
                st32c(__float_as_uint(cb), rws, lane * 4 + 256, OFF_CSP + (j * 8 + TT) * 512);
                wait_vm<0>();
                if (lane == 0) set_flag(ws, OFF_FWJ + (j * 8 + TT) * 128, tag);
            } else {
                wait_vm<10>();
                if (lane == 0) set_flag(ws, OFF_FWC + (c * 32 + t) * 128, tag);
                wait_vm<1>();
                if (lane == 0) set_flag(ws, OFF_FW4 + (c * 32 + t) * 128, tag);
                lds_wait_ge(L_CNT, 4u);
                __builtin_amdgcn_s_sleep(PA_DELAY);
                poll(ws, OFF_FA + (hp * 16 + l15) * 128, tag);
            }
            STAMP(22);
        }
        bar();
        STAMP(7);
        if (wv < 7) {
            gather();
        } else {
            if (lane < 5) lds_st128(L_ZR + lane * 336, v4u{0u, 0u, 0u, 0u});
            poll(ws, lane < 32 ? OFF_FW4 + (c * 32 + lane) * 128 : OFF_FWJ + (j * 8 + (lane & 7)) * 128, tag);
        }
        bar_lds();
        STAMP(8);
        const int hw = wv - 4;
        const int k0 = hw < 2 ? 14 * hw : 28 + 13 * (hw - 2), sk = hw < 2 ? 0 : 1;
        const unsigned wb4 = OFF_W4 + c * 276480 + k0 * 5120, hb = OFF_HH + 4 * t * 55296 + k0 * 1024;
        const unsigned l16x = hw < 2 ? l16 : OOB;
        const int le = hw < 3 ? hw * 20480 : 122880, ll = hw < 3 ? 61440 + hw * 20480 : 143360;
        unsigned leb = le + l16, llb = ll + l16;
        asm volatile("" : "+v"(leb), "+v"(llb));
        v4u wr[6][5], wl[20], hr0, hr1; v2u hr2;
        auto w4ld = [&](auto f0, auto f1, auto pace) {
#pragma unroll
            for (int f = decltype(f0)::value; f < decltype(f1)::value; ++f) {
                if (f < 30) wr[f / 5][f % 5] = ld128c(rws, f >= 25 ? l16x : l16, wb4 + f * 1024);
                else wl[f - 30] = ld128c(rws, l16, wb4 + (10 - sk) * 5120 + (f - 30) * 1024);
                asm volatile("s_sleep %0" ::"n"(decltype(pace)::value) : "memory");
            }
        };
        __builtin_amdgcn_s_sleep(B_SLEEP);
        {
            const unsigned v3 = j < 31 ? l16 : OOB, w3b = OFF_W3 + j * 143360;
            if (hw == 0) { dma_run<15>(rws, w2frag(84), l16, OFF_W2 + c * 102400 + 86016); dma_c(rws, w2frag(99), l16, OFF_W2 + c * 102400 + 101376); }
            else if (hw == 1) dma_run<21>(rws, w3slot(0), v3, w3b);
            else dma_run<14>(rws, w3slot(3) + (hw - 2) * 14336, v3, w3b + 21504 + (hw - 2) * 14336);
        }
        w4ld(ic<0>{}, ic<W4_N1>{}, ic<0>{});
        if (hw == 3) {
            lds_wait_ge(L_CNT, 8u);
            __builtin_amdgcn_s_sleep(PB_DELAY);
            poll(ws, OFF_FB + ((t >> 1) * 16 + l15) * 128, tag);
            if (b == 0 && lane == 0) set_flag(ws, OFF_EPOCH, tag);
        } else {
            wait_vm<W4_N1>();
        }
        STAMP(26);
        bar();
        if (hw == 3) {
            float s0 = 0.f, s1 = 0.f;
#pragma unroll
            for (int i = 0; i < 8; ++i) {
                s0 += __uint_as_float(ld32c(rws, lane * 4, OFF_CSP + (j * 8 + i) * 512));
                s1 += __uint_as_float(ld32c(rws, lane * 4 + 256, OFF_CSP + (j * 8 + i) * 512));
            }
            *lp<float>(L_CS + lane * 4) = s0;
            if (lane < 48) *lp<float>(L_CS + 256 + lane * 4) = s1;
            __builtin_amdgcn_s_sleep(P2_SLEEP / 2);
        } else {
            __builtin_amdgcn_s_sleep(P2_SLEEP);
        }
        w4ld(ic<W4_N1>{}, ic<W4_N1 + W4_N2>{}, ic<0>{});
        lds_wait_ge(L_W2D + (lane & 3) * 4, 1u);
        const unsigned v3 = j < 31 ? l16 : OOB, w3r = OFF_W3 + j * 143360 + 50176 + hw * 1024;
        auto w3rest = [&](int i) {
            if (i < 9) { if (i < 8 || hw < 3) dma_c(rws, w3slot(7) + hw * 1024 + i * 4096, v3, w3r + i * 4096); }
            else dma_c(rws, w3slot(12) + hw * 1024 + (i - 9) * 4096, v3, w3r + 35840 + (i - 9) * 4096);
        };
#pragma unroll
        for (int i = 0; i < 9; ++i) w3rest(i);
        wait_vm<0>();
        STAMP(36);
        if (hw == 3) {
            lds_wait_ge(L_CNT, 12u);
            __builtin_amdgcn_s_sleep(PC_DELAY);
            poll(ws, OFF_FC + (TT * 32 + l31) * 128, tag);
        }
        STAMP(28);
        bar_lds();
        STAMP(12);
        {
            __builtin_amdgcn_s_sleep(W4_SLEEP);
#pragma unroll
            for (int i = 9; i < 23; ++i) {
                w3rest(i);
                asm volatile("s_sleep %0" ::"n"(W3_PACE) : "memory");
                if (i == 20) { wait_vm<5>(); *lp<volatile unsigned>(L_W3R + hw * 4) = 2u; }
            }
            wait_vm<0>();
            *lp<volatile unsigned>(L_W3R + hw * 4) = 3u;
            STAMP(35);
            w4ld(ic<W4_N1 + W4_N2>{}, ic<50>{}, ic<W4_PACE>{});
            const unsigned ho = OFF_HB + (4 * t + hw) * 20480 + 10 * c * 256 + l15 * 16;
            hr0 = ld128c(rws, ho + lg * 512, 0); hr1 = ld128c(rws, ho + lg * 512 + 256, 0); hr2 = ld64c(rws, ho + (8 + (lg >> 1)) * 256 + (lg & 1) * 8, 0);
            STAMP(34);
            if (j < 31) lds_wait_ge(L_PRG + (lane & 3) * 4, 1u);
            dma_run<20>(rws, le, l16, wb4 + (6 - sk) * 5120);
            if (hw == 3) poll(ws, OFF_FD + ((t * 2 + 0) * 32 + (l31 < 31 ? l31 : 30)) * 128, tag);
            STAMP(31);
            bar();
            STAMP(15);
#pragma unroll
            for (int f = 0; f < 20; ++f) lds_st128(llb + f * 1024, wl[f]);
            wait_vm<0>();
        }
        STAMP(30);
        f32x4 acc[2][5];
        v4u fh[5];
        auto run = [&](auto kc, auto pdc) {
            constexpr int k = decltype(kc)::value, PD = decltype(pdc)::value;
            v4u a[28], w[5];
            auto hld = [&](int f) {
                const int n = f >> 1;
                if (f < 28) a[f] = ld128c(rws, n == 5 ? l16x : l16, hb + (k + (f & 1)) * 55296 + (n < 6 ? n : n - sk) * 1024);
            };
#pragma unroll
            for (int f = 0; f < 2 * PD; ++f) hld(f);
            if (k == 0) bar();
#pragma unroll
            for (int n = 0; n < 14; ++n) {
#pragma unroll
                for (int u = 0; u < 5; ++u) {
                    const v4u wf = n < 6 ? wr[n < 6 ? n : 0][u] : w[u];
                    mma16f(n == 0, acc[0][u], wf, a[2 * n]);
                    if (n > 0 && (u == 0 || u == 2)) hld(2 * (n - 1 + PD) + (u == 2));
                    mma16f(n == 0, acc[1][u], wf, a[2 * n + 1]);
                    if (n >= 5 && n < 13) { const int f = (n - 5) * 5 + u; w[u] = f < 20 ? lds128(leb + f * 1024) : lds128(llb + (f - 20) * 1024); }
                    if (k == 2 && n == 10) lds_st128(leb + u * 1024, fh[u]);
                }
            }
            mma_end(acc[0]); mma_end(acc[1]);
        };
        run(ic<0>{}, ic<P4_PD1>{});
#pragma unroll
        for (int u = 0; u < 5; ++u) {
            fh[u] = v4u{pk2v(acc[0][u].xy), pk2v(acc[0][u].zw), pk2v(acc[1][u].xy), pk2v(acc[1][u].zw)};
            asm volatile("" : "+v"(fh[u]));
        }
        STAMP(16);
        bar();
        STAMP(17); CSTAMP(55);
        run(ic<2>{}, ic<P4_PD2>{});
        STAMP(18); CSTAMP(56);
#pragma unroll
        for (int u = 0; u < 5; ++u) lds_st128(leb + (5 + u) * 1024, v4u{pk2v(acc[0][u].xy), pk2v(acc[0][u].zw), pk2v(acc[1][u].xy), pk2v(acc[1][u].zw)});
        bar_lds();
        {
            const unsigned hk[10] = {hr0.x, hr0.y, hr0.z, hr0.w, hr1.x, hr1.y, hr1.z, hr1.w, hr2.x, hr2.y};
            const int ra = (hw >> 1) * 5120 + l16 + (hw & 1) * 8;
            unsigned y[10];
#pragma unroll
            for (int u = 0; u < 5; ++u) {
                f32x2 r0 = {bflo(hk[2 * u]), bfhi(hk[2 * u])}, r1 = {bflo(hk[2 * u + 1]), bfhi(hk[2 * u + 1])};
#pragma unroll
                for (int k = 0; k < 4; ++k) {
                    const v2u p = *lp<v2u>(ra + (k < 3 ? k * 20480 : 122880) + u * 1024);
                    r0 += f32x2{bflo(p.x), bfhi(p.x)}; r1 += f32x2{bflo(p.y), bfhi(p.y)};
                }
                y[2 * u] = pk2v(r0); y[2 * u + 1] = pk2v(r1);
            }
            const rsrc_t ro = mk_rsrc(out, SEQ * DM * 2);
            const unsigned o = ((64 * t + 16 * hw + l15) * 640 + 80 * c + 20 * lg) * 2;
            __builtin_amdgcn_raw_buffer_store_b128(v4u{y[0], y[1], y[2], y[3]}, ro, o, 0, 0);
            __builtin_amdgcn_raw_buffer_store_b128(v4u{y[4], y[5], y[6], y[7]}, ro, o + 16, 0, 0);
            __builtin_amdgcn_raw_buffer_store_b64(v2u{y[8], y[9]}, ro, o + 32, 0, 0);
        }
        STAMP(19);
    }
    STAMP(61);
    if (b == 0 && wv == 0) { const u64 t0 = __builtin_amdgcn_s_memrealtime(); u64 dl = (ampb - ampa) >> 1; if (dl > 100000) dl = 100000; while (__builtin_amdgcn_s_memrealtime() - t0 < dl) {} }
}

}

extern "C" void kernel_launch(void* const* d_in, const int* in_sizes, int n_in, void* d_out, int out_size, void* d_ws, size_t ws_size,
                              hipStream_t stream) {
    (void)in_sizes; (void)n_in; (void)out_size; (void)ws_size;
    fused_block<<<256, 512, 0, stream>>>((const u16*)d_in[0], (const u16*)d_in[1], (const u16*)d_in[2], (const u16*)d_in[3],
                                         (const u16*)d_in[5], (const u16*)d_in[6], (const u16*)d_in[7], (u16*)d_out, (char*)d_ws);
}
```

```cpp
#include <hip/hip_runtime.h>
#include <stdint.h>

typedef unsigned short u16;
typedef unsigned long long u64;
typedef unsigned v4u __attribute__((ext_vector_type(4)));
typedef unsigned v2u __attribute__((ext_vector_type(2)));
typedef float f32x2 __attribute__((ext_vector_type(2)));
typedef float f32x4 __attribute__((ext_vector_type(4)));
typedef float f32x16 __attribute__((ext_vector_type(16)));
typedef __bf16 bf16x2 __attribute__((ext_vector_type(2)));
typedef __bf16 bf16x8 __attribute__((ext_vector_type(8)));
typedef short s16x4 __attribute__((ext_vector_type(4)));
typedef __amdgpu_buffer_rsrc_t rsrc_t;
#define GAS __attribute__((address_space(1)))
#define LAS __attribute__((address_space(3)))
#define DI __device__ __forceinline__

namespace {

constexpr int SEQ = 2048, DM = 640, FF = 1728;

constexpr unsigned OFF_EPOCH = 0;
constexpr unsigned OFF_F0  = 4096;
constexpr unsigned OFF_FA  = OFF_F0 + 32768;
constexpr unsigned OFF_FB  = OFF_FA + 32768;
constexpr unsigned OFF_FC  = OFF_FB + 32768;
constexpr unsigned OFF_FWC = OFF_FC + 32768;
constexpr unsigned OFF_FWJ = OFF_FWC + 32768;
constexpr unsigned OFF_FW4 = OFF_FWJ + 32768;
constexpr unsigned OFF_FD  = OFF_FW4 + 32768;
constexpr unsigned OFF_W1  = OFF_FD + 262144;
constexpr unsigned OFF_C1P = OFF_W1 + 16 * 163840;
constexpr unsigned OFF_KVP = OFF_C1P + 256 * 640;
constexpr unsigned OFF_O   = OFF_KVP + 256 * 3584;
constexpr unsigned OFF_W2  = OFF_O + 128 * 20480;
constexpr unsigned OFF_HB  = OFF_W2 + 8 * 100 * 1024;
constexpr unsigned OFF_ST  = OFF_HB + 128 * 20480;
constexpr unsigned OFF_CSP = OFF_ST + 128 * 8 * 128;
constexpr unsigned OFF_W3  = OFF_CSP + 32 * 8 * 512;
constexpr unsigned OFF_HH  = OFF_W3 + 31 * 140 * 1024;
constexpr unsigned OFF_W4  = OFF_HH + 128 * 216 * 256;
constexpr unsigned WS_USED = OFF_W4 + 8 * 270 * 1024;
constexpr unsigned OFF_STAMP = (WS_USED + 4095) & ~4095u;
constexpr unsigned OOB = 0x7ffffff0u;

constexpr int LDS_BYTES = 163840;
constexpr int L_X    = 0;
constexpr int L_F    = 131072;
constexpr int L_PF   = 152576;
constexpr int L_RG = 0, L_RU = L_RG + 9216, L_RD = L_RU + 9216, L_RO = L_RD + 9600;
constexpr int L_W1Q  = 122880;
constexpr int L_PKV  = 40960;
constexpr int L_KVT  = 153600;
constexpr int L_ZR   = L_KVT + 3584;
constexpr int L_CS   = L_ZR + 1376;
constexpr int L_C1   = L_CS + 512;
constexpr int L_CNT  = 163584;
constexpr int L_PRG = L_CNT + 16, L_W3R = L_CNT + 32, L_W2D = L_CNT + 48, L_TG = L_CNT + 64;
__host__ __device__ constexpr int w2frag(int f) { return f < 40 ? f * 1024 : f < 84 ? 77824 + (f - 40) * 1024 : f < 99 ? 62464 + (f - 84) * 1024 : 151552; }
__host__ __device__ constexpr int w3slot(int i) { return i < 3 ? 40960 + i * 7168 : i < 7 ? 122880 + (i - 3) * 7168 : i < 12 ? (i - 7) * 7168 : 62464 + (i - 12) * 7168; }

#define CODEPF 0
#define RAW_SLEEP 0
#define W4_SLEEP 12
#define P0_DELAY 12
#define PA_DELAY 14
#define PB_DELAY 14
#define PC_DELAY 14
#define W4_N1 0
#define W4_N2 10
#define D_SLEEP 14
#define RT_SLEEP 6
#define W3_PACE 1
#define W4_PACE 2
#define P3_FLAG_AT 4
#define P3_LPS 4
#define P4_PD1 6
#define P4_PD2 6
#define MAPY 1
#define ROT 0
#define P0_PRIO 3
#define XRING 6
#define A_SLEEP 12
#define B_SLEEP 0
#define C_SLEEP 4
#define P3_PDB 18
#define P3_PACE 4
#define P1_FLAG_AT 20
#define P2_PD 12
#define P2_SLEEP 24
#define P3_PD 7
#define P3_SLEEP 40
#define POLL_SLEEP 1
constexpr int AUX_SC1 = 16;
constexpr float LOG2E = 1.4426950408889634f;

DI rsrc_t mk_rsrc(const void* p, unsigned bytes) { return __builtin_amdgcn_make_buffer_rsrc((void*)p, 0, bytes, 0x00020000); }
DI v4u ld128c(rsrc_t r, unsigned v, unsigned s) { return __builtin_amdgcn_raw_buffer_load_b128(r, v, s, AUX_SC1); }
DI v2u ld64c(rsrc_t r, unsigned v, unsigned s) { return __builtin_amdgcn_raw_buffer_load_b64(r, v, s, AUX_SC1); }
DI unsigned ld32c(rsrc_t r, unsigned v, unsigned s) { return __builtin_amdgcn_raw_buffer_load_b32(r, v, s, AUX_SC1); }
DI v4u ld128p(rsrc_t r, unsigned v, unsigned s) { return __builtin_amdgcn_raw_buffer_load_b128(r, v, s, 0); }
DI v2u ld64p(rsrc_t r, unsigned v, unsigned s) { return __builtin_amdgcn_raw_buffer_load_b64(r, v, s, 0); }
DI void st128c(v4u d, rsrc_t r, unsigned v, unsigned s) { __builtin_amdgcn_raw_buffer_store_b128(d, r, v + s, 0, AUX_SC1); }
DI void st64c(v2u d, rsrc_t r, unsigned v, unsigned s) { __builtin_amdgcn_raw_buffer_store_b64(d, r, v, s, AUX_SC1); }
DI void st32c(unsigned d, rsrc_t r, unsigned v, unsigned s) { __builtin_amdgcn_raw_buffer_store_b32(d, r, v, s, AUX_SC1); }

DI void dma_c(rsrc_t r, unsigned lds, unsigned v, unsigned s) {
    asm volatile("s_mov_b32 m0, %0\n\ts_nop 0\n\tbuffer_load_dwordx4 %1, %2, %3 offen sc1 lds" ::"s"(lds), "v"(v), "s"(r), "s"(s) : "memory");
}
DI void dma_p(rsrc_t r, unsigned lds, unsigned v, unsigned s) {
    asm volatile("s_mov_b32 m0, %0\n\ts_nop 0\n\tbuffer_load_dwordx4 %1, %2, %3 offen lds" ::"s"(lds), "v"(v), "s"(r), "s"(s) : "memory");
}
template <int N> DI void dma_run(rsrc_t r, unsigned lds, unsigned v, unsigned s) {
    if constexpr (N >= 4) {
        asm volatile("s_mov_b32 m0, %0\n\ts_nop 0\n\tbuffer_load_dwordx4 %1, %2, %3 offen sc1 lds\n\tbuffer_load_dwordx4 %1, %2, %3 offen offset:1024 sc1 lds\n\t"
                     "buffer_load_dwordx4 %1, %2, %3 offen offset:2048 sc1 lds\n\tbuffer_load_dwordx4 %1, %2, %3 offen offset:3072 sc1 lds" ::"s"(lds), "v"(v), "s"(r), "s"(s) : "memory");
        dma_run<N - 4>(r, lds + 4096, v, s + 4096);
    } else if constexpr (N > 0) {
        dma_c(r, lds, v, s);
        dma_run<N - 1>(r, lds + 1024, v, s + 1024);
    }
}
DI void warm(rsrc_t r, unsigned lds, unsigned v, unsigned s) {
    asm volatile("s_mov_b32 m0, %0\n\ts_nop 0\n\tbuffer_load_dword %1, %2, %3 offen sc1 lds" ::"s"(lds), "v"(v), "s"(r), "s"(s) : "memory");
}
template <int N> DI void wait_vm() { asm volatile("s_waitcnt vmcnt(%0)" ::"n"(N) : "memory"); }
DI void wait_lds() { asm volatile("s_waitcnt lgkmcnt(0)" ::: "memory"); }
DI void bar() { asm volatile("s_barrier" ::: "memory"); }
DI void bar_lds() { asm volatile("s_waitcnt lgkmcnt(0)\n\ts_barrier" ::: "memory"); }
template <int N, int EXTRA> DI void drain_steps() { wait_vm<6 * N + EXTRA>(); bar(); if constexpr (N > 0) drain_steps<N - 1, EXTRA>(); }

template <int N> struct ic { static constexpr int value = N; };
template <class T> DI LAS T* lp(int off) { return (LAS T*)(size_t)(unsigned)off; }
DI v4u lds128(int off) { return *lp<v4u>(off); }
DI void lds_st128(int off, v4u d) { *lp<v4u>(off) = d; }
DI void lds_st64(int off, v2u d) { *lp<v2u>(off) = d; }
DI v2u lds_tr(int off) { return __builtin_bit_cast(v2u, __builtin_amdgcn_ds_read_tr16_b64_v4i16(lp<s16x4>(off))); }
DI v4u lds_tr2(int a0, int a1) { const v2u x0 = lds_tr(a0), x1 = lds_tr(a1); return v4u{x0.x, x0.y, x1.x, x1.y}; }

DI f32x16 mfma32(v4u a, v4u b, f32x16 c) { return __builtin_amdgcn_mfma_f32_32x32x16_bf16(__builtin_bit_cast(bf16x8, a), __builtin_bit_cast(bf16x8, b), c, 0, 0, 0); }
DI f32x4 mfma16(v4u a, v4u b, f32x4 c) { return __builtin_amdgcn_mfma_f32_16x16x32_bf16(__builtin_bit_cast(bf16x8, a), __builtin_bit_cast(bf16x8, b), c, 0, 0, 0); }
template <bool FIRST> DI void mma16(f32x4& c, v4u a, v4u b) {
    if constexpr (FIRST) asm volatile("v_mfma_f32_16x16x32_bf16 %0, %1, %2, 0" : "=&v"(c) : "v"(a), "v"(b) : "memory");
    else asm volatile("v_mfma_f32_16x16x32_bf16 %0, %1, %2, %0" : "+v"(c) : "v"(a), "v"(b) : "memory");
}
template <bool FIRST> DI void mma32(f32x16& c, v4u a, v4u b) {
    if constexpr (FIRST) asm volatile("v_mfma_f32_32x32x16_bf16 %0, %1, %2, 0" : "=&v"(c) : "v"(a), "v"(b) : "memory");
    else asm volatile("v_mfma_f32_32x32x16_bf16 %0, %1, %2, %0" : "+v"(c) : "v"(a), "v"(b) : "memory");
}
DI void mma16f(bool first, f32x4& c, v4u a, v4u b) { if (first) mma16<true>(c, a, b); else mma16<false>(c, a, b); }
DI void mma32f(bool first, f32x16& c, v4u a, v4u b) { if (first) mma32<true>(c, a, b); else mma32<false>(c, a, b); }
DI void mma_end(f32x16& a) { asm volatile("s_nop 7\n\ts_nop 4" : "+v"(a)); }
DI void mma_end(f32x16& a, f32x16& b, f32x16& c) { asm volatile("s_nop 7\n\ts_nop 4" : "+v"(a), "+v"(b), "+v"(c)); }
DI void mma_end(f32x4 (&a)[5]) { asm volatile("s_nop 7\n\ts_nop 0" : "+v"(a[0]), "+v"(a[1]), "+v"(a[2]), "+v"(a[3]), "+v"(a[4])); }
DI void mma_end(f32x4 (&a)[7]) { asm volatile("s_nop 7\n\ts_nop 0" : "+v"(a[0]), "+v"(a[1]), "+v"(a[2]), "+v"(a[3]), "+v"(a[4]), "+v"(a[5]), "+v"(a[6])); }
struct Frag {
    unsigned b0, b1, b2;
    DI explicit Frag(unsigned l16) : b0(l16), b1(l16 + 65536), b2(l16 + 131072) { asm volatile("" : "+v"(b0), "+v"(b1), "+v"(b2)); }
    DI v4u operator()(int off) const { return off < 65536 ? lds128(b0 + off) : off < 131072 ? lds128(b1 + (off - 65536)) : lds128(b2 + (off - 131072)); }
};
DI unsigned pk2v(f32x2 p) { bf16x2 q = __builtin_convertvector(p, bf16x2); return __builtin_bit_cast(unsigned, q); }
DI unsigned pk2(float lo, float hi) { return pk2v(f32x2{lo, hi}); }
DI float bflo(unsigned u) { return __uint_as_float(u << 16); }
DI float bfhi(unsigned u) { return __uint_as_float(u & 0xffff0000u); }
DI float dot2(unsigned a, unsigned b, float c) { return __builtin_amdgcn_fdot2_f32_bf16(__builtin_bit_cast(bf16x2, a), __builtin_bit_cast(bf16x2, b), c, false); }
DI float sum8(v4u v, float a) { unsigned x0 = v.x, x1 = v.y, x2 = v.z, x3 = v.w; return dot2(x0, 0x3f803f80u, dot2(x1, 0x3f803f80u, dot2(x2, 0x3f803f80u, dot2(x3, 0x3f803f80u, a)))); }
DI float sum_x16(float v) { unsigned u = __float_as_uint(v); auto r = __builtin_amdgcn_permlane16_swap(u, u, false, false); unsigned a = r[0], b = r[1]; return __uint_as_float(a) + __uint_as_float(b); }
DI float sum_x32(float v) { unsigned u = __float_as_uint(v); auto r = __builtin_amdgcn_permlane32_swap(u, u, false, false); unsigned a = r[0], b = r[1]; return __uint_as_float(a) + __uint_as_float(b); }
DI void swap16(unsigned& a, unsigned& b) { auto r = __builtin_amdgcn_permlane16_swap(a, b, false, false); a = r[0]; b = r[1]; }
DI void swap32(unsigned& a, unsigned& b) { auto r = __builtin_amdgcn_permlane32_swap(a, b, false, false); a = r[0]; b = r[1]; }
DI void swap32f(float& a, float& b) { unsigned x = __float_as_uint(a), y = __float_as_uint(b); swap32(x, y); a = __uint_as_float(x); b = __uint_as_float(y); }
DI f32x2 exp2x2(f32x2 v) { return f32x2{__builtin_amdgcn_exp2f(v.x), __builtin_amdgcn_exp2f(v.y)}; }
DI f32x2 fma2(f32x2 a, f32x2 b, f32x2 c) { return __builtin_elementwise_fma(a, b, c); }
DI f32x2 sp2(float v) { return f32x2{v, v}; }

DI void set_flag(char* ws, unsigned off, u64 tag) { __hip_atomic_store((GAS u64*)(ws + off), tag, __ATOMIC_RELAXED, __HIP_MEMORY_SCOPE_AGENT); }
DI void arrive4(int cnt, int lane, char* ws, unsigned off, u64 tag) {
    if (lane == 0) {
        const unsigned old = __hip_atomic_fetch_add(lp<unsigned>(cnt), 1u, __ATOMIC_RELAXED, __HIP_MEMORY_SCOPE_WORKGROUP);
        if ((old & 3u) == 3u) set_flag(ws, off, tag);
    }
}
DI void lds_wait(int off, u64 tag) {
    for (int sp = 0; sp < (1 << 22); ++sp)
        if (__all(*lp<volatile u64>(off) == tag)) break; else __builtin_amdgcn_s_sleep(1);
    asm volatile("" ::: "memory");
}
DI void poll(char* ws, unsigned off, u64 tag) {
    GAS u64* p = (GAS u64*)(ws + off);
    for (int sp = 0; sp < (1 << 17); ++sp) {
        if (__hip_atomic_load(p, __ATOMIC_RELAXED, __HIP_MEMORY_SCOPE_AGENT) == tag) break;
        if (POLL_SLEEP) __builtin_amdgcn_s_sleep(POLL_SLEEP);
    }
    __builtin_amdgcn_fence(__ATOMIC_ACQUIRE, "wavefront");
    asm volatile("" ::: "memory");
}
DI void lds_wait_ge(int off, unsigned n) {
    for (int sp = 0; sp < (1 << 22); ++sp)
        if (__all(*lp<volatile unsigned>(off) >= n)) break; else __builtin_amdgcn_s_sleep(1);
    asm volatile("" ::: "memory");
}

DI void feature_map(const f32x2 (&z)[10], float rs, unsigned (&o)[20]) {
    float mx = fmaxf(z[0].x, z[0].y), mn = fminf(z[0].x, z[0].y);
#pragma unroll
    for (int i = 1; i < 10; ++i) { mx = fmaxf(fmaxf(mx, z[i].x), z[i].y); mn = fminf(fminf(mn, z[i].x), z[i].y); }
    f32x2 ep[10], en[10];
#pragma unroll
    for (int i = 0; i < 10; ++i) { ep[i] = exp2x2(fma2(z[i], sp2(rs), sp2(-rs * mx))); en[i] = exp2x2(fma2(z[i], sp2(-rs), sp2(rs * mn))); }
    f32x2 sp = ep[0], sn = en[0];
#pragma unroll
    for (int i = 1; i < 10; ++i) { sp += ep[i]; sn += en[i]; }
    const float rp = __builtin_amdgcn_rcpf(sp.x + sp.y), rn = __builtin_amdgcn_rcpf(sn.x + sn.y);
#pragma unroll
    for (int i = 0; i < 10; ++i) { o[i] = pk2v(ep[i] * rp); o[10 + i] = pk2v(en[i] * rn); }
}

DI f32x2 geglu2(f32x2 G, f32x2 U, float K1, float K2, float IV) {
    const f32x2 e = exp2x2(G * fma2(G * G, sp2(K2), sp2(K1)));
    const f32x2 d = fma2(e, sp2(IV), sp2(IV));
    return G * U * f32x2{__builtin_amdgcn_rcpf(d.x), __builtin_amdgcn_rcpf(d.y)};
}
DI void p3_epilogue(const f32x4 (&acc)[2][7], const f32x4 (&cs)[7], const v2u (&st)[2][2], unsigned (&hh)[2][7]) {
    float c6[4] = {cs[6].x, cs[6].y, cs[6].z, cs[6].w};
    swap32f(c6[0], c6[2]); swap32f(c6[1], c6[3]);
#pragma unroll
    for (int k = 0; k < 2; ++k) {
        const float s = sum_x32(sum_x16(__uint_as_float(st[k][0].x) + __uint_as_float(st[k][1].x)));
        const float q = sum_x32(sum_x16(__uint_as_float(st[k][0].y) + __uint_as_float(st[k][1].y)));
        const float nm = s * (-1.f / 640.f), IV = fmaxf(q * (1.f / 640.f) - nm * nm, 0.f) + 1e-5f;
        const float rs = __builtin_amdgcn_rsqf(IV);
        const float K1 = (-2.f * 0.7978845608028654f * LOG2E) * rs, K2 = (0.044715f * K1) * (rs * rs);
        float z6[4] = {acc[k][6].x, acc[k][6].y, acc[k][6].z, acc[k][6].w};
        swap32f(z6[0], z6[2]); swap32f(z6[1], z6[3]);
#pragma unroll
        for (int u = 0; u < 3; ++u) {
            hh[k][2 * u] = pk2v(geglu2(fma2(cs[u].xy, sp2(nm), acc[k][u].xy), fma2(cs[u + 3].xy, sp2(nm), acc[k][u + 3].xy), K1, K2, IV));
            hh[k][2 * u + 1] = pk2v(geglu2(fma2(cs[u].zw, sp2(nm), acc[k][u].zw), fma2(cs[u + 3].zw, sp2(nm), acc[k][u + 3].zw), K1, K2, IV));
        }
        hh[k][6] = pk2v(geglu2(fma2(f32x2{c6[0], c6[1]}, sp2(nm), f32x2{z6[0], z6[1]}), fma2(f32x2{c6[2], c6[3]}, sp2(nm), f32x2{z6[2], z6[3]}), K1, K2, IV));
    }
}

#define STAMP(i) do {} while (0)
#define CSTAMP(i) do {} while (0)
#define SB() __builtin_amdgcn_sched_barrier(0)

__global__ __launch_bounds__(512, 2) void fused_block(const u16* __restrict__ x, const u16* __restrict__ wqkv, const u16* __restrict__ wphi,
                                                      const u16* __restrict__ wo, const u16* __restrict__ wg, const u16* __restrict__ wu,
                                                      const u16* __restrict__ wd, u16* __restrict__ out, char* __restrict__ ws) {
    __shared__ __attribute__((aligned(16))) char smem[LDS_BYTES];
    asm volatile("" ::"v"((LAS char*)smem));
    STAMP(0); CSTAMP(62);
    asm volatile("" ::"s"(x), "s"(wqkv), "s"(wphi), "s"(wo), "s"(wg), "s"(wu), "s"(wd), "s"(out), "s"(ws));
    const int tid = threadIdx.x, lane = tid & 63;
    const int wv = __builtin_amdgcn_readfirstlane(tid >> 6);
    const int b = blockIdx.x, xcd = b & 7, rk = b >> 3;
    u64 pc; unsigned clen;
    asm volatile("s_getpc_b64 %0\nfb_code_lo:\n\ts_mov_b32 %1, fb_code_hi-fb_code_lo" : "=s"(pc), "=s"(clen));
    auto codepf = [&]() {
        const rsrc_t rc = mk_rsrc((const void*)pc, clen);
        const unsigned v = __builtin_amdgcn_raw_buffer_load_b32(rc, lane < 20 ? (lane * 32 + rk) * 128 : OOB, 0, 0);
        asm volatile("" ::"v"(v));
    };
    if (wv == 3) {
        const rsrc_t rc = mk_rsrc((const void*)pc, clen);
        if (lane < 20) asm volatile("s_mov_b32 m0, %0\n\ts_nop 0\n\tbuffer_load_dword %1, %2, 0 offen lds" ::"s"(L_PF), "v"((lane * 32 + rk) * 128), "s"(rc) : "memory");
        if (lane < 16) *lp<unsigned>(L_CNT + lane * 4) = 0u;
    }
    const int m = (xcd >> 1) * 4 + (rk >> 3), hp = (xcd & 1) * 8 + (rk & 7);
    const int t = (xcd >> 1) * 8 + (rk >> 2), c = (xcd & 1) * 4 + (rk & 3);
    const int TT = (xcd >> 2) * 4 + (rk >> 3), j = (xcd & 3) * 8 + (rk & 7);
    const rsrc_t rws = mk_rsrc(ws, WS_USED);
    const int l15 = lane & 15, lg = lane >> 4, l31 = lane & 31, lh = lane >> 5;
    const unsigned l16 = lane * 16;
    const v2u ep = ld64c(rws, OFF_EPOCH, 0);
    u64 tag;
    auto gather = [&]() {
        v4u g[8];
        const unsigned go = (32 * wv + l31) * 16 + lh * 8 * 3584;
#pragma unroll
        for (int i = 0; i < 8; ++i) g[i] = ld128c(rws, go, OFF_KVP + (hp * 16 + ((i + 2 * m) & 7)) * 3584);
        SB();
        f32x2 v[4] = {};
#pragma unroll
        for (int i = 0; i < 8; ++i)
#pragma unroll
            for (int e = 0; e < 4; ++e) { const unsigned u = g[i][e]; v[e] += f32x2{bflo(u), bfhi(u)}; }
        float z[8] = {v[0].x, v[0].y, v[1].x, v[1].y, v[2].x, v[2].y, v[3].x, v[3].y};
#pragma unroll
        for (int e = 0; e < 4; ++e) { swap32f(z[e], z[e + 4]); z[e] += z[e + 4]; }
        lds_st64(L_KVT + (32 * wv + l31) * 16 + lh * 8, v2u{pk2(z[0], z[1]), pk2(z[2], z[3])});
    };
    if (wv < 4) {
        unsigned phq[20];
        v4u xr0, xr1; v2u xr2;
        {
        v4u a[40];
        float mean1, rs1;
        {
            const rsrc_t rx = mk_rsrc(x, SEQ * DM * 2);
            const int lx = L_X + wv * 32768;
            unsigned vo[2];
#pragma unroll
            for (int g = 0; g < 2; ++g) vo[g] = (lane >> 3) * 1280 + (((lane & 7) ^ ((4 * g + (lane >> 4)) & 7)) * 16);
            const unsigned so = (128 * m + 32 * wv) * 1280;
            auto blk = [&](int bb) {
#pragma unroll
                for (int g = 0; g < 4; ++g) dma_p(rx, lx + (bb % 8) * 4096 + g * 1024, vo[g & 1], so + g * 10240 + bb * 128);
            };
#pragma unroll
            for (int bb = 0; bb < 8; ++bb) blk(bb);
            STAMP(20);
            unsigned ra[4];
#pragma unroll
            for (int s = 0; s < 4; ++s) ra[s] = lx + l31 * 128 + (((2 * s + lh) ^ ((l31 >> 1) & 7)) * 16);
            float s0 = 0.f, s1 = 0.f, q0 = 0.f, q1 = 0.f;
            auto rd = [&](int bb) {
#pragma unroll
                for (int s = 0; s < 4; ++s) a[4 * bb + s] = lds128(ra[s] + (bb % 8) * 4096);
            };
            auto stat = [&](int bb) {
#pragma unroll
                for (int s = 0; s < 4; ++s) {
                    const unsigned e0 = a[4 * bb + s].x, e1 = a[4 * bb + s].y, e2 = a[4 * bb + s].z, e3 = a[4 * bb + s].w;
                    s0 = dot2(e0, 0x3f803f80u, s0); q0 = dot2(e0, e0, q0); s1 = dot2(e1, 0x3f803f80u, s1); q1 = dot2(e1, e1, q1);
                    s0 = dot2(e2, 0x3f803f80u, s0); q0 = dot2(e2, e2, q0); s1 = dot2(e3, 0x3f803f80u, s1); q1 = dot2(e3, e3, q1);
                }
                SB();
            };
            wait_vm<24>(); STAMP(21); rd(0); rd(1); wait_lds(); blk(8); blk(9);
            STAMP(45);
            wait_vm<24>(); rd(2); rd(3); SB(); stat(0); stat(1); stat(2); stat(3);
            wait_vm<16>(); rd(4); rd(5); SB(); stat(4); stat(5);
            wait_vm<8>(); rd(6); rd(7); SB(); stat(6); stat(7);
            wait_vm<0>(); rd(8); rd(9); SB(); stat(8); stat(9);
            const float s = sum_x32(s0 + s1), q = sum_x32(q0 + q1);
            mean1 = s * (1.f / 640.f);
            rs1 = __builtin_amdgcn_rsqf(fmaxf(q * (1.f / 640.f) - mean1 * mean1, 0.f) + 1e-5f);
        }
        tag = (((u64)__builtin_amdgcn_readfirstlane(ep.y) << 32) | __builtin_amdgcn_readfirstlane(ep.x)) + 1;
        STAMP(1);
        bar_lds();
        STAMP(3);
        f32x16 acc[4];
        const Frag frag(l16);
        bar();
        STAMP(40); CSTAMP(50);
        v4u w[6];
#pragma unroll
        for (int u = 0; u < 6; ++u) w[u] = frag(u * 1024);
        SB();
#pragma unroll
        for (int i = 0; i < 40; ++i) {
            if (i == 6 || i == 12 || i == 26) bar();
#pragma unroll
            for (int u = 0; u < 3; ++u) {
                mma32f(i == 0, acc[u], w[(i & 1) * 3 + u], a[i]);
                if (i + 2 < 40) w[(i & 1) * 3 + u] = frag(((i + 2) * 3 + u) * 1024);
            }
        }
        mma_end(acc[0], acc[1], acc[2]);
        STAMP(4); CSTAMP(51);
        lds_wait(L_TG + 32, tag);
        auto centred = [&](int idx0, f32x2 (&z)[10]) {
#pragma unroll
            for (int i = 0; i < 5; ++i) {
                const f32x4 cs = *lp<f32x4>(L_C1 + lh * 320 + (idx0 + 4 * i) * 4);
                const int e = idx0 + 4 * i;
                z[2 * i] = fma2(cs.xy, sp2(-mean1), f32x2{acc[e >> 4][e & 15], acc[e >> 4][(e & 15) + 1]});
                z[2 * i + 1] = fma2(cs.zw, sp2(-mean1), f32x2{acc[e >> 4][(e & 15) + 2], acc[e >> 4][(e & 15) + 3]});
            }
        };
        {
            f32x2 z[10]; unsigned pk[20];
            const int ro = L_PKV + (lh * 128 + 32 * wv + l31) * 144;
            centred(20, z);
#pragma unroll
            for (int i = 0; i < 10; ++i) pk[i] = pk2v(z[i] * rs1);
            lds_st128(ro + 80, v4u{pk[0], pk[1], pk[2], pk[3]});
            lds_st128(ro + 96, v4u{pk[4], pk[5], pk[6], pk[7]});
            lds_st128(ro + 112, v4u{pk[8], pk[9], 0x3f80u, 0u});
            centred(0, z);
            feature_map(z, rs1, pk);
#pragma unroll
            for (int i = 0; i < 5; ++i) lds_st128(ro + i * 16, v4u{pk[4 * i], pk[4 * i + 1], pk[4 * i + 2], pk[4 * i + 3]});
        }
        STAMP(22); CSTAMP(57);
        bar_lds();
        {
            const int head = wv >> 1, ft = wv & 1, blk = lg & 1, q = l15 >> 2, p = lane & 3;
            const int rb = L_PKV + (head * 128 + 8 * lh + q) * 144;
            const int ao = rb + (32 * ft + 16 * blk + 8 * (p & 1) + 4 * (p >> 1)) * 2, bo = rb + (40 + 16 * blk + 4 * p) * 2;
            f32x16 d = {};
            v4u ta[4], tb[4];
#pragma unroll
            for (int s = 0; s < 4; ++s) { ta[s] = lds_tr2(ao + s * 2304, ao + s * 2304 + 576); tb[s] = lds_tr2(bo + s * 2304, bo + s * 2304 + 576); }
            SB();
#pragma unroll
            for (int s = 0; s < 8; ++s) {
                d = mfma32(ta[s & 3], tb[s & 3], d);
                if (s < 4) { ta[s] = lds_tr2(ao + (s + 4) * 2304, ao + (s + 4) * 2304 + 576); tb[s] = lds_tr2(bo + (s + 4) * 2304, bo + (s + 4) * 2304 + 576); }
                SB();
            }
            const v4u c0 = {pk2(d[0], d[1]), pk2(d[2], d[3]), pk2(d[4], d[5]), pk2(d[6], d[7])};
            const v4u c1 = {pk2(d[8], d[9]), pk2(d[10], d[11]), pk2(d[12], d[13]), pk2(d[14], d[15])};
            const unsigned o = OFF_KVP + (hp * 16 + m) * 3584 + ((head * 5 + 4 * ft + lh) * 21 + l31) * 16;
            st128c(c0, rws, (l31 < 21 && (ft == 0 || lh == 0)) ? o : OOB, 0);
            st128c(c1, rws, (l31 < 21 && ft == 0) ? o + 2 * 21 * 16 : OOB, 0);
        }
        STAMP(23);
        unsigned lq = L_W1Q + (lh * 24 + (l31 < 24 ? l31 : l31 - 8)) * 16;
        asm volatile("" : "+v"(lq));
#pragma unroll
        for (int u = 0; u < 6; ++u) w[u] = lds128(lq + u * 768);
        SB();
#pragma unroll
        for (int i = 0; i < 40; ++i) {
            if (i == P1_FLAG_AT) { wait_vm<0>(); arrive4(L_CNT, lane, ws, OFF_FA + (hp * 16 + m) * 128, tag); STAMP(5); }
            mma32f(i == 0, acc[3], w[i % 6], a[i]);
            if (i + 6 < 40) w[i % 6] = lds128(lq + (i + 6) * 768);
        }
        mma_end(acc[3]);
        STAMP(24);
        {
            const rsrc_t rx = mk_rsrc(x, SEQ * DM * 2);
            const unsigned o = ((64 * t + 16 * wv + l15) * 640 + 80 * c + 20 * lg) * 2;
            xr0 = ld128p(rx, o, 0); xr1 = ld128p(rx, o + 16, 0); xr2 = ld64p(rx, o + 32, 0);
        }
        f32x2 z[10];
        centred(40, z);
        feature_map(z, rs1, phq);
#pragma unroll
        for (int i = 0; i < 20; ++i) asm volatile("" : "+v"(phq[i]));
        STAMP(6);
        }
        bar();
        STAMP(7);
        gather();
        bar_lds();
        STAMP(8);
        unsigned hk[10];
        {
            const int i16 = (l31 & 3) + 4 * (l31 >> 3);
            const bool mine = ((l31 >> 2) & 1) == lh;
            const int a0 = mine ? L_KVT + (lh * 105 + i16) * 16 : L_ZR, a1 = mine ? L_KVT + (lh * 105 + 16 + (i16 < 5 ? i16 : 4)) * 16 : L_ZR;
            v4u k0[5], k1[5];
#pragma unroll
            for (int s = 0; s < 5; ++s) { k1[s] = lds128(a1 + s * 336); k0[s] = lds128(a0 + s * 336); }
            SB();
            f32x16 d0 = {}, d1 = {};
#pragma unroll
            for (int s = 0; s < 5; ++s) {
                const v4u bq = {phq[4 * s], phq[4 * s + 1], phq[4 * s + 2], phq[4 * s + 3]};
                d1 = mfma32(k1[s], bq, d1);
                d0 = mfma32(k0[s], bq, d0);
            }
            const float rd = __builtin_amdgcn_rcpf(d1[4] + 1e-6f);
            const v4u c0 = {pk2(d0[0] * rd, d0[1] * rd), pk2(d0[2] * rd, d0[3] * rd), pk2(d0[4] * rd, d0[5] * rd), pk2(d0[6] * rd, d0[7] * rd)};
            const v4u c1 = {pk2(d0[8] * rd, d0[9] * rd), pk2(d0[10] * rd, d0[11] * rd), pk2(d0[12] * rd, d0[13] * rd), pk2(d0[14] * rd, d0[15] * rd)};
            const v2u c2 = {pk2(d1[0] * rd, d1[1] * rd), pk2(d1[2] * rd, d1[3] * rd)};
            const unsigned o = OFF_O + (8 * m + 2 * wv + (l31 >> 4)) * 20480 + 5 * hp * 256 + l15 * 16;
            st128c(c0, rws, o + lh * 512, 0);
            st128c(c1, rws, o + lh * 512 + 256, 0);
            st64c(c2, rws, o + 1024 + lh * 8, 0);
        }
        STAMP(25);
        {
        const Frag frag(l16);
        v4u w[30];
#pragma unroll
        for (int f = 0; f < 30; ++f) w[f] = frag(w2frag(f));
        wait_vm<0>();
        arrive4(L_CNT, lane, ws, OFF_FB + (m * 16 + hp) * 128, tag);
        STAMP(9);
        bar();
        STAMP(10);
        v4u a[20];
        const unsigned oo = OFF_O + (4 * t + wv) * 20480;
        auto old = [&](int i) { a[i] = ld128c(rws, l16, oo + i * 1024); };
        constexpr int PD = P2_PD;
#pragma unroll
        for (int i = 0; i < PD; ++i) old(i);
        SB();
        f32x4 acc[5];
#pragma unroll
        for (int f = 0; f < 100; ++f) {
            if (f % 5 == 0 && f / 5 + PD < 20) old(f / 5 + PD);
            mma16f(f < 5, acc[f % 5], w[f % 30], a[f / 5]);
            if (f + 30 < 100) w[f % 30] = frag(w2frag(f + 30));
        }
        *lp<volatile unsigned>(L_W2D + wv * 4) = 1u;
        mma_end(acc);
        STAMP(26);
        const unsigned xr[10] = {xr0.x, xr0.y, xr0.z, xr0.w, xr1.x, xr1.y, xr1.z, xr1.w, xr2.x, xr2.y};
        float s = 0.f, q = 0.f;
#pragma unroll
        for (int i = 0; i < 10; ++i) {
            hk[i] = pk2(acc[i >> 1][2 * (i & 1)] + bflo(xr[i]), acc[i >> 1][2 * (i & 1) + 1] + bfhi(xr[i]));
            s = dot2(hk[i], 0x3f803f80u, s); q = dot2(hk[i], hk[i], q);
        }
        const unsigned o = OFF_HB + (4 * t + wv) * 20480 + 10 * c * 256 + l15 * 16;
        st128c(v4u{hk[0], hk[1], hk[2], hk[3]}, rws, o + lg * 512, 0);
        st128c(v4u{hk[4], hk[5], hk[6], hk[7]}, rws, o + lg * 512 + 256, 0);
        st64c(v2u{hk[8], hk[9]}, rws, o + (8 + (lg >> 1)) * 256 + (lg & 1) * 8, 0);
        s = sum_x32(sum_x16(s)); q = sum_x32(sum_x16(q));
        st64c(v2u{__float_as_uint(s), __float_as_uint(q)}, rws, lane < 16 ? OFF_ST + ((4 * t + wv) * 8 + c) * 128 + lane * 8 : OOB, 0);
        STAMP(27);
        wait_vm<0>();
        arrive4(L_CNT, lane, ws, OFF_FC + (t * 8 + c) * 128, tag);
        STAMP(11);
        }
        bar();
        STAMP(12); CSTAMP(52);
        if (j < 31) {
            const int tt0 = 16 * TT + 4 * wv;
            const unsigned ho = OFF_HB + tt0 * 20480;
            const Frag frag(l16);
            constexpr int PD = P3_PD;
            v4u a[80], w[7];
            v2u st[2][2][2];
            unsigned rdy = 0;
            auto hld = [&](int f) { if (f < 80) a[f] = ld128c(rws, l16, ho + ((f / 40) * 2 + (f & 1)) * 20480 + ((f % 40) >> 1) * 1024); };
            auto stld = [&](int e) { st[e >> 2][(e >> 1) & 1][e & 1] = ld64c(rws, (2 * lg + (e & 1)) * 128 + l15 * 8, OFF_ST + (tt0 + (e >> 1)) * 1024); };
#pragma unroll
            for (int f = 0; f < 2 * PD; ++f) hld(f);
#pragma unroll
            for (int u = 0; u < 7; ++u) w[u] = frag(w3slot(0) + u * 1024);
#pragma unroll
            for (int ps = 0; ps < 2; ++ps) {
                f32x4 acc[2][7];
#pragma unroll
                for (int i = 0; i < 20; ++i) {
                    if (ps == 0 && (i == 11 || i == 15)) {
                        const unsigned need = i == 11 ? 2u : 3u;
                        if (!__all(rdy >= need)) {
                            for (int sp = 0; sp < (1 << 22); ++sp)
                                if (__all(*lp<volatile unsigned>(L_W3R + (lane & 3) * 4) >= need)) break;
                        }
                        asm volatile("" ::: "memory");
                    }
                    if (ps == 1 && i == 12) *lp<volatile unsigned>(L_PRG + wv * 4) = 1u;
                    if (ps == 1 && i == P3_FLAG_AT) {
                        wait_vm<2 * P3_FLAG_AT>();
                        if (lane == 0) set_flag(ws, OFF_FD + (((4 * TT + wv) * 2) * 32 + j) * 128, tag);
                    }
#pragma unroll
                    for (int u = 0; u < 7; ++u) {
                        mma16f(i == 0, acc[0][u], w[u], a[40 * ps + 2 * i]);
                        if (u == 2 || u == 5) hld(40 * ps + 2 * (i + PD) + (u == 5));
                        if (u == 0 && i >= 8 && i < 12) stld(4 * ps + i - 8);
                        if (ps == 0 && u == 3 && (i == 10 || i == 14)) rdy = *lp<volatile unsigned>(L_W3R + (lane & 3) * 4);
                        mma16f(i == 0, acc[1][u], w[u], a[40 * ps + 2 * i + 1]);
                        if (i + 1 < 20 || ps == 0) w[u] = frag(w3slot((i + 1) % 20) + u * 1024);
                    }
                }
                mma_end(acc[0]); mma_end(acc[1]);
                if (ps == 0) { STAMP(32); CSTAMP(53); } else { STAMP(13); CSTAMP(58); }
                f32x4 cs[7];
#pragma unroll
                for (int u = 0; u < 7; ++u) cs[u] = *lp<f32x4>(L_CS + (u * 16 + lg * 4) * 4);
                if (ps == 1) bar_lds();
                unsigned hh[2][7];
                p3_epilogue(acc, cs, st[ps], hh);
                const unsigned o = OFF_HH + (tt0 + 2 * ps) * 55296 + 7 * j * 256 + l15 * 16;
                st128c(v4u{hh[0][0], hh[0][1], hh[0][2], hh[0][3]}, rws, o + lg * 256, 0);
                st128c(v4u{hh[1][0], hh[1][1], hh[1][2], hh[1][3]}, rws, o + lg * 256, 55296);
                swap16(hh[0][4], hh[1][4]); swap16(hh[0][5], hh[1][5]); swap16(hh[0][6], hh[1][6]);
                st128c(v4u{hh[0][4], hh[0][5], hh[1][4], hh[1][5]}, rws, o + (lg & 1) * 55296 + (4 + (lg >> 1)) * 256, 0);
                st64c(v2u{hh[0][6], hh[1][6]}, rws, j < 30 ? o + (lg & 1) * 55296 + 6 * 256 + (lg >> 1) * 8 : OOB, 0);
                asm volatile("" ::: "memory");
                if (ps == 0) { STAMP(33); CSTAMP(54); } else { STAMP(29); CSTAMP(59); }
            }
            bar();
            wait_vm<0>();
            if (lane == 0) set_flag(ws, OFF_FD + (((4 * TT + wv) * 2 + 1) * 32 + j) * 128, tag);
            STAMP(14);
        } else {
            bar();
            bar();
        }
        if (wv == 0) {
            __builtin_amdgcn_s_sleep(D_SLEEP);
            poll(ws, OFF_FD + ((t * 2 + 1) * 32 + (l31 < 31 ? l31 : 30)) * 128, tag);
        }
        bar();
        bar();
        asm volatile("fb_code_hi:");
    } else {
        {
            const int isq = (wv - 4) >> 1, fhead = wv & 1, lf = L_F + (wv - 4) * 5120;
            if (P0_PRIO) __builtin_amdgcn_s_setprio(P0_PRIO);
            const rsrc_t rq = mk_rsrc(wqkv, DM * 3 * DM * 2), rp = mk_rsrc(wphi, 32 * 400 * 2);
            const unsigned so = (40 * m * 1920 + (2 * hp + fhead) * 20) * 2;
            if (lane < 63) {
                const unsigned vo = (lane / 3) * 3840 + (lane % 3) * 16;
                dma_p(rq, lf, vo, so + (isq ? 0 : 1280)); dma_p(rq, lf + 1008, lane < 57 ? vo : OOB, so + (isq ? 0 : 1280) + 21 * 3840);
                dma_p(rp, lf + 2016, lane < 50 ? l16 : OOB, (2 * hp + fhead) * 800);
                dma_p(rq, lf + 3024, (isq && lane >= 48) ? OOB : vo, so + 2560 + (isq ? 24 * 3840 : 0));
                dma_p(rq, lf + 4032, (isq || lane >= 9) ? OOB : vo, so + 2560 + 21 * 3840);
            }
            const int r = (l31 & 3) + 4 * (l31 >> 3), hh = (l31 >> 2) & 1;
            const int a1 = lf + (16 * (r >> 3) + 8 * hh + (r & 7)) * 48, a2 = lf + (32 + (r & 7)) * 48;
            const int tc = (16 * (lg & 1) + 4 * (lane & 3)) * 2;
            const int pa = lf + 2016 + (l15 >> 2) * 40 + tc, va = lf + 3024 + (8 * lh + (l15 >> 2)) * 48 + tc;
            const unsigned ob = OFF_W1 + hp * 163840 + (5 * m + lh) * 2048 + fhead * 1024 + l31 * 16;
            const unsigned o = l31 < 20 ? ob + (isq ? 640 : 0) : OOB, ov = l31 < 20 ? ob + 320 + (isq ? 6144 : 0) : OOB;
            STAMP(20);
            wait_vm<0>();
            STAMP(21);
            const v4u fa0 = lds128(a1 + 16 * lh), fc0 = lds128(a2 + 16 * lh);
            const v2u fa1 = *lp<v2u>(a1 + 32), fc1 = *lp<v2u>(a2 + 32);
            const v4u b0 = lds_tr2(pa + lh * 320, pa + lh * 320 + 160);
            const v2u p2 = lds_tr(pa + 640);
            const v4u v0 = lds_tr2(va, va + 192), v1 = lds_tr2(va - lh * 384 + 768, va - lh * 384 + 960);
            const v4u b1 = {lh == 0 ? p2.x : 0u, lh == 0 ? p2.y : 0u, 0u, 0u};
            f32x16 d = {}, e = {};
            d = mfma32(fa0, b0, d);
            e = mfma32(fc0, b0, e);
            d = mfma32(v4u{fa1.x, fa1.y, 0u, 0u}, b1, d);
            e = mfma32(v4u{fc1.x, fc1.y, 0u, 0u}, b1, e);
            const bool third = lh == 0 && !isq;
            st128c(v0, rws, ov, 0);
            st128c(v1, rws, third ? ov : OOB, 4096);
            const float sv = sum_x32(sum8(v0, third ? sum8(v1, 0.f) : 0.f));
            const float sc = LOG2E;
            const v4u c0 = {pk2(d[0] * sc, d[1] * sc), pk2(d[2] * sc, d[3] * sc), pk2(d[4] * sc, d[5] * sc), pk2(d[6] * sc, d[7] * sc)};
            const v4u c1 = {pk2(d[8] * sc, d[9] * sc), pk2(d[10] * sc, d[11] * sc), pk2(d[12] * sc, d[13] * sc), pk2(d[14] * sc, d[15] * sc)};
            const v4u c2 = {pk2(e[0] * sc, e[1] * sc), pk2(e[2] * sc, e[3] * sc), pk2(e[4] * sc, e[5] * sc), pk2(e[6] * sc, e[7] * sc)};
            st128c(c0, rws, o, 0);
            st128c(c1, rws, o, 4096);
            st128c(c2, rws, lh == 0 ? o : OOB, 8192);
            const float sw = sum_x32(sum8(c0, sum8(c1, lh == 0 ? sum8(c2, 0.f) : 0.f)));
            st32c(__float_as_uint(lh == 0 ? sw : sv), rws, l31 < 20 ? OFF_C1P + (hp * 16 + m) * 640 + fhead * 320 + (isq * 40 + lh * 20 + l31) * 4 : OOB, 0);
        }
        tag = (((u64)__builtin_amdgcn_readfirstlane(ep.y) << 32) | __builtin_amdgcn_readfirstlane(ep.x)) + 1;
        STAMP(1);
        wait_vm<0>();
        *lp<volatile u64>(L_TG + (wv - 4) * 8) = tag;
        if (wv == 4) {
            lds_wait(L_TG + (lane & 3) * 8, tag);
            if (lane == 0) set_flag(ws, OFF_F0 + (hp * 16 + m) * 128, tag);
            STAMP(2);
            __builtin_amdgcn_s_sleep(P0_DELAY);
            poll(ws, OFF_F0 + (hp * 16 + l15) * 128, tag);
        }
        if (P0_PRIO) __builtin_amdgcn_s_setprio(0);
        bar();
        STAMP(3);
        const unsigned wb1 = OFF_W1 + hp * 163840;
        if (wv < 6) {
            const int lw = wv - 4, i16 = (l31 & 3) + 4 * (l31 >> 3);
            const unsigned wl = lh * 2048 + ((l31 >> 2) & 1) * 1024 + i16 * 16;
            auto issue = [&](int ch) {
#pragma unroll
                for (int u = 0; u < 6; ++u) dma_c(rws, ((4 * ch + 2 * lw) * 3 + u) * 1024, wl, wb1 + (4 * ch + 2 * lw + u / 3) * 4096 + (u % 3) * 256);
            };
            issue(0); issue(1); issue(2); issue(3); wait_vm<12>(); bar();
            issue(4); issue(5); issue(6); wait_vm<18>(); bar();
            issue(7); issue(8); issue(9); wait_vm<18>(); bar();
            wait_vm<0>(); bar();
            {
                const int r3 = lane < 24 ? lane : lane - 24;
                const unsigned wl3 = (lane < 24 ? 0 : 2048) + ((r3 >> 2) & 1) * 1024 + (48 + (r3 & 3) + 4 * (r3 >> 3)) * 16;
                if (lane < 48) {
#pragma unroll
                    for (int i = 0; i < 20; ++i) dma_c(rws, L_W1Q + (2 * i + lw) * 768, wl3, wb1 + (2 * i + lw) * 4096);
                }
                wait_vm<0>();
            }
            STAMP(22);
            bar();
            __builtin_amdgcn_s_sleep(A_SLEEP);
            poll(ws, OFF_FWC + (c * 32 + l15) * 128, tag);
            STAMP(23);
            dma_run<40>(rws, lw * 77824, l16, OFF_W2 + c * 102400 + lw * 40960);
            dma_run<2>(rws, w2frag(80) + lw * 2048, l16, OFF_W2 + c * 102400 + 81920 + lw * 2048);
            STAMP(24);
        } else {
            bar();
            if (wv == 6) {
                v4u cp[16];
#pragma unroll
                for (int i = 0; i < 16; ++i) cp[i] = ld128c(rws, lane < 40 ? l16 : OOB, OFF_C1P + (hp * 16 + i) * 640);
                bar();
#pragma unroll
                for (int i = 0; i < 16; ++i) asm volatile("" : "+v"(cp[i]));
                bar();
                const rsrc_t rg = mk_rsrc(wg, DM * FF * 2), ru = mk_rsrc(wu, DM * FF * 2);
                const unsigned vo = (lane / 7) * (FF * 2) + (lane % 7) * 16, so = (80 * TT * FF + 56 * j) * 2;
                if (lane < 63) {
#pragma unroll
                    for (int i = 0; i < 9; ++i) { dma_p(rg, L_RG + i * 1008, vo, so + i * 9 * FF * 2); dma_p(ru, L_RU + i * 1008, vo, so + i * 9 * FF * 2); }
                }
                float c1s[4];
#pragma unroll
                for (int e = 0; e < 4; ++e) { const unsigned u = cp[0][e]; c1s[e] = __uint_as_float(u); }
#pragma unroll
                for (int i = 1; i < 16; ++i) {
                    const unsigned u0 = cp[i].x, u1 = cp[i].y, u2 = cp[i].z, u3 = cp[i].w;
                    asm("v_add_f32 %0, %0, %4\n\tv_add_f32 %1, %1, %5\n\tv_add_f32 %2, %2, %6\n\tv_add_f32 %3, %3, %7"
                        : "+v"(c1s[0]), "+v"(c1s[1]), "+v"(c1s[2]), "+v"(c1s[3]) : "v"(u0), "v"(u1), "v"(u2), "v"(u3));
                }
#pragma unroll
                for (int e = 0; e < 4; ++e) {
                    float o2 = __shfl(c1s[e], lane + 10);
                    o2 = ((lane >= 5 && lane < 10) || (lane >= 25 && lane < 30)) ? o2 : 0.f;
                    asm("v_add_f32 %0, %0, %1" : "+v"(c1s[e]) : "v"(o2));
                }
                if (lane < 40) *lp<f32x4>(L_C1 + l16) = f32x4{c1s[0], c1s[1], c1s[2], c1s[3]};
                wait_lds();
                *lp<volatile u64>(L_TG + 32) = tag;
            } else {
                bar(); bar();
                const rsrc_t rd = mk_rsrc(wd, DM * FF * 2), ro = mk_rsrc(wo, DM * DM * 2);
                const unsigned vo = (lane / 10) * (DM * 2) + (lane % 10) * 16;
                if (lane < 60) {
#pragma unroll
                    for (int i = 0; i < 10; ++i) dma_p(rd, L_RD + i * 960, vo, (56 * t * DM + 80 * c) * 2 + i * 6 * DM * 2);
#pragma unroll
                    for (int i = 0; i < 7; ++i) dma_p(ro, L_RO + i * 960, vo, (40 * t * DM + 80 * c) * 2 + i * 6 * DM * 2);
                }
            }
            STAMP(41);
            bar();
            const int q = l15 >> 2, p = lane & 3;
            v4u rt[18];
            unsigned dv[18];
            wait_vm<0>();
            STAMP(42);
            if (wv == 6) {
                const int ra = L_RG + (20 * lg + q) * 112, c6 = (p >> 1) * 9216 + (48 + 4 * (p & 1)) * 2;
                const int rb = L_RG + (40 * (lg & 1) + 16 + q) * 112;
#pragma unroll
                for (int hf = 0; hf < 2; ++hf) {
#pragma unroll
                    for (int ti = 0; ti < 6; ++ti) { const int a = ra + 24 * p + hf * 896 + (ti / 3) * 9216 + (ti % 3) * 8; rt[hf * 7 + ti] = lds_tr2(a, a + 448); }
                    rt[hf * 7 + 6] = lds_tr2(ra + c6 + hf * 896, ra + c6 + hf * 896 + 448);
                }
#pragma unroll
                for (int s = 0; s < 3; ++s) { const int a = rb + (lg >> 1) * 9216 + 24 * p + s * 8; rt[14 + s] = lds_tr2(a, a + 2240); }
                rt[17] = lds_tr2(rb + c6, rb + c6 + 2240);
                const bool on = j < 31;
                auto dst = [&](int Q) { return OFF_W3 + (j * 20 + (Q >> 2)) * 7168 + ((Q & 3) * 16 + l15) * 16; };
                const unsigned d0 = on ? dst(10 * TT + 2 * lg) : OOB, d1 = on ? dst(10 * TT + 2 * lg + 1) : OOB, d2 = on ? dst(10 * TT + 8 + (lg & 1)) : OOB;
#pragma unroll
                for (int ti = 0; ti < 7; ++ti) { dv[ti] = d0 + ti * 1024; dv[7 + ti] = d1 + ti * 1024; }
#pragma unroll
                for (int s = 0; s < 3; ++s) dv[14 + s] = d2 + (lg >> 1) * 3072 + s * 1024;
                dv[17] = lg < 2 ? d2 + 6144 : OOB;
            } else {
                const int ra = L_RD + (12 * lg + q) * 160 + 40 * p;
                const int r0 = lg < 2 ? 24 * lg + 8 + q : 48 + 4 * (q >> 1) + (q & 1), r1 = lg < 2 ? r0 + 12 : r0 + 2;
                const int oa = L_RO + (20 * (lg >> 1) + 8 * (lg & 1) + q) * 160 + 40 * p, ob = L_RO + (16 + q) * 160 + 40 * p;
                auto d4 = [&](int Q) { return Q < 216 ? OFF_W4 + (c * 54 + (Q >> 2)) * 5120 + ((Q & 3) * 16 + l15) * 16 : OOB; };
                auto d2 = [&](int Q) { return t < 16 ? OFF_W2 + (c * 20 + (Q >> 2)) * 5120 + ((Q & 3) * 16 + l15) * 16 : OOB; };
                const unsigned da = d4(7 * t + lg), db = lg < 3 ? d4(7 * t + 4 + lg) : OOB, dc = d2(5 * t + lg), dd = d2(5 * t + 4);
#pragma unroll
                for (int ti = 0; ti < 5; ++ti) {
                    rt[ti] = lds_tr2(ra + ti * 8, ra + ti * 8 + 640);
                    rt[5 + ti] = lds_tr2(L_RD + r0 * 160 + 40 * p + ti * 8, L_RD + r1 * 160 + 40 * p + ti * 8);
                    rt[10 + ti] = lds_tr2(oa + ti * 8, oa + ti * 8 + 640);
                    dv[ti] = da + ti * 1024; dv[5 + ti] = db + ti * 1024; dv[10 + ti] = dc + ti * 1024;
                }
                rt[15] = lds_tr2(ob + lg * 8, ob + lg * 8 + 3200);
                rt[16] = lds_tr2(ob + 32, ob + 32 + 3200);
                rt[17] = rt[16];
                dv[15] = dd + lg * 1024; dv[16] = lg == 0 ? dd + 4096 : OOB; dv[17] = OOB;
            }
            STAMP(43);
#pragma unroll
            for (int i = 17; i >= 0; --i) st128c(rt[i], rws, dv[i], 0);
            STAMP(44);
            bar();
            if (wv == 6) {
                float cs[8];
#pragma unroll
                for (int ti = 0; ti < 7; ++ti) cs[ti] = sum8(rt[ti], sum8(rt[7 + ti], 0.f));
#pragma unroll
                for (int s = 0; s < 3; ++s) { const float e = sum8(rt[14 + s], 0.f); cs[s] += lg < 2 ? e : 0.f; cs[s + 3] += lg < 2 ? 0.f : e; }
                { const float e = sum8(rt[17], 0.f); cs[6] += lg < 2 ? e : 0.f; }
#pragma unroll
                for (int ti = 0; ti < 7; ++ti) cs[ti] = sum_x32(sum_x16(cs[ti]));
                cs[7] = 0.f;
                const float ca = lg == 0 ? cs[0] : lg == 1 ? cs[1] : lg == 2 ? cs[2] : cs[3], cb = lg == 0 ? cs[4] : lg == 1 ? cs[5] : lg == 2 ? cs[6] : cs[7];
                st32c(__float_as_uint(ca), rws, lane * 4, OFF_CSP + (j * 8 + TT) * 512);
                st32c(__float_as_uint(cb), rws, lane * 4 + 256, OFF_CSP + (j * 8 + TT) * 512);
                wait_vm<0>();
                if (lane == 0) set_flag(ws, OFF_FWJ + (j * 8 + TT) * 128, tag);
            } else {
                wait_vm<10>();
                if (lane == 0) set_flag(ws, OFF_FWC + (c * 32 + t) * 128, tag);
                wait_vm<1>();
                if (lane == 0) set_flag(ws, OFF_FW4 + (c * 32 + t) * 128, tag);
                lds_wait_ge(L_CNT, 4u);
                __builtin_amdgcn_s_sleep(PA_DELAY);
                poll(ws, OFF_FA + (hp * 16 + l15) * 128, tag);
            }
            STAMP(22);
        }
        bar();
        STAMP(7);
        if (wv < 7) {
            gather();
        } else {
            if (lane < 5) lds_st128(L_ZR + lane * 336, v4u{0u, 0u, 0u, 0u});
            poll(ws, lane < 32 ? OFF_FW4 + (c * 32 + lane) * 128 : OFF_FWJ + (j * 8 + (lane & 7)) * 128, tag);
        }
        bar_lds();
        STAMP(8);
        const int hw = wv - 4;
        const int k0 = hw < 2 ? 14 * hw : 28 + 13 * (hw - 2), sk = hw < 2 ? 0 : 1;
        const unsigned wb4 = OFF_W4 + c * 276480 + k0 * 5120, hb = OFF_HH + 4 * t * 55296 + k0 * 1024;
        const unsigned l16x = hw < 2 ? l16 : OOB;
        const int le = hw < 3 ? hw * 20480 : 122880, ll = hw < 3 ? 61440 + hw * 20480 : 143360;
        unsigned leb = le + l16, llb = ll + l16;
        asm volatile("" : "+v"(leb), "+v"(llb));
        v4u wr[6][5], wl[20], hr0, hr1; v2u hr2;
        auto w4ld = [&](auto f0, auto f1, auto pace) {
#pragma unroll
            for (int f = decltype(f0)::value; f < decltype(f1)::value; ++f) {
                if (f < 30) wr[f / 5][f % 5] = ld128c(rws, f >= 25 ? l16x : l16, wb4 + f * 1024);
                else wl[f - 30] = ld128c(rws, l16, wb4 + (10 - sk) * 5120 + (f - 30) * 1024);
                asm volatile("s_sleep %0" ::"n"(decltype(pace)::value) : "memory");
            }
        };
        __builtin_amdgcn_s_sleep(B_SLEEP);
        {
            const unsigned v3 = j < 31 ? l16 : OOB, w3b = OFF_W3 + j * 143360;
            if (hw == 0) { dma_run<15>(rws, w2frag(84), l16, OFF_W2 + c * 102400 + 86016); dma_c(rws, w2frag(99), l16, OFF_W2 + c * 102400 + 101376); }
            else if (hw == 1) dma_run<21>(rws, w3slot(0), v3, w3b);
            else dma_run<14>(rws, w3slot(3) + (hw - 2) * 14336, v3, w3b + 21504 + (hw - 2) * 14336);
        }
        w4ld(ic<0>{}, ic<W4_N1>{}, ic<0>{});
        if (hw == 3) {
            lds_wait_ge(L_CNT, 8u);
            __builtin_amdgcn_s_sleep(PB_DELAY);
            poll(ws, OFF_FB + ((t >> 1) * 16 + l15) * 128, tag);
            if (b == 0 && lane == 0) set_flag(ws, OFF_EPOCH, tag);
        } else {
            wait_vm<W4_N1>();
        }
        STAMP(26);
        bar();
        if (hw == 3) {
            float s0 = 0.f, s1 = 0.f;
#pragma unroll
            for (int i = 0; i < 8; ++i) {
                s0 += __uint_as_float(ld32c(rws, lane * 4, OFF_CSP + (j * 8 + i) * 512));
                s1 += __uint_as_float(ld32c(rws, lane * 4 + 256, OFF_CSP + (j * 8 + i) * 512));
            }
            *lp<float>(L_CS + lane * 4) = s0;
            if (lane < 48) *lp<float>(L_CS + 256 + lane * 4) = s1;
            __builtin_amdgcn_s_sleep(P2_SLEEP / 2);
        } else {
            __builtin_amdgcn_s_sleep(P2_SLEEP);
        }
        w4ld(ic<W4_N1>{}, ic<W4_N1 + W4_N2>{}, ic<0>{});
        lds_wait_ge(L_W2D + (lane & 3) * 4, 1u);
        const unsigned v3 = j < 31 ? l16 : OOB, w3r = OFF_W3 + j * 143360 + 50176 + hw * 1024;
        auto w3rest = [&](int i) {
            if (i < 9) { if (i < 8 || hw < 3) dma_c(rws, w3slot(7) + hw * 1024 + i * 4096, v3, w3r + i * 4096); }
            else dma_c(rws, w3slot(12) + hw * 1024 + (i - 9) * 4096, v3, w3r + 35840 + (i - 9) * 4096);
        };
#pragma unroll
        for (int i = 0; i < 9; ++i) w3rest(i);
        wait_vm<0>();
        STAMP(36);
        if (hw == 3) {
            lds_wait_ge(L_CNT, 12u);
            __builtin_amdgcn_s_sleep(PC_DELAY);
            poll(ws, OFF_FC + (TT * 32 + l31) * 128, tag);
        }
        STAMP(28);
        bar_lds();
        STAMP(12);
        {
            __builtin_amdgcn_s_sleep(W4_SLEEP);
#pragma unroll
            for (int i = 9; i < 23; ++i) {
                w3rest(i);
                asm volatile("s_sleep %0" ::"n"(W3_PACE) : "memory");
                if (i == 20) { wait_vm<5>(); *lp<volatile unsigned>(L_W3R + hw * 4) = 2u; }
            }
            wait_vm<0>();
            *lp<volatile unsigned>(L_W3R + hw * 4) = 3u;
            STAMP(35);
            w4ld(ic<W4_N1 + W4_N2>{}, ic<50>{}, ic<W4_PACE>{});
            const unsigned ho = OFF_HB + (4 * t + hw) * 20480 + 10 * c * 256 + l15 * 16;
            hr0 = ld128c(rws, ho + lg * 512, 0); hr1 = ld128c(rws, ho + lg * 512 + 256, 0); hr2 = ld64c(rws, ho + (8 + (lg >> 1)) * 256 + (lg & 1) * 8, 0);
            STAMP(34);
            if (j < 31) lds_wait_ge(L_PRG + (lane & 3) * 4, 1u);
            dma_run<20>(rws, le, l16, wb4 + (6 - sk) * 5120);
            if (hw == 3) poll(ws, OFF_FD + ((t * 2 + 0) * 32 + (l31 < 31 ? l31 : 30)) * 128, tag);
            STAMP(31);
            bar();
            STAMP(15);
#pragma unroll
            for (int f = 0; f < 20; ++f) lds_st128(llb + f * 1024, wl[f]);
            wait_vm<0>();
        }
        STAMP(30);
        f32x4 acc[2][5];
        v4u fh[5];
        auto run = [&](auto kc, auto pdc) {
            constexpr int k = decltype(kc)::value, PD = decltype(pdc)::value;
            v4u a[28], w[5];
            auto hld = [&](int f) {
                const int n = f >> 1;
                if (f < 28) a[f] = ld128c(rws, n == 5 ? l16x : l16, hb + (k + (f & 1)) * 55296 + (n < 6 ? n : n - sk) * 1024);
            };
#pragma unroll
            for (int f = 0; f < 2 * PD; ++f) hld(f);
            if (k == 0) bar();
#pragma unroll
            for (int n = 0; n < 14; ++n) {
#pragma unroll
                for (int u = 0; u < 5; ++u) {
                    const v4u wf = n < 6 ? wr[n < 6 ? n : 0][u] : w[u];
                    mma16f(n == 0, acc[0][u], wf, a[2 * n]);
                    if (n > 0 && (u == 0 || u == 2)) hld(2 * (n - 1 + PD) + (u == 2));
                    mma16f(n == 0, acc[1][u], wf, a[2 * n + 1]);
                    if (n >= 5 && n < 13) { const int f = (n - 5) * 5 + u; w[u] = f < 20 ? lds128(leb + f * 1024) : lds128(llb + (f - 20) * 1024); }
                    if (k == 2 && n == 10) lds_st128(leb + u * 1024, fh[u]);
                }
            }
            mma_end(acc[0]); mma_end(acc[1]);
        };
        run(ic<0>{}, ic<P4_PD1>{});
#pragma unroll
        for (int u = 0; u < 5; ++u) {
            fh[u] = v4u{pk2v(acc[0][u].xy), pk2v(acc[0][u].zw), pk2v(acc[1][u].xy), pk2v(acc[1][u].zw)};
            asm volatile("" : "+v"(fh[u]));
        }
        STAMP(16);
        bar();
        STAMP(17); CSTAMP(55);
        run(ic<2>{}, ic<P4_PD2>{});
        STAMP(18); CSTAMP(56);
#pragma unroll
        for (int u = 0; u < 5; ++u) lds_st128(leb + (5 + u) * 1024, v4u{pk2v(acc[0][u].xy), pk2v(acc[0][u].zw), pk2v(acc[1][u].xy), pk2v(acc[1][u].zw)});
        bar_lds();
        {
            const unsigned hk[10] = {hr0.x, hr0.y, hr0.z, hr0.w, hr1.x, hr1.y, hr1.z, hr1.w, hr2.x, hr2.y};
            const int ra = (hw >> 1) * 5120 + l16 + (hw & 1) * 8;
            unsigned y[10];
#pragma unroll
            for (int u = 0; u < 5; ++u) {
                f32x2 r0 = {bflo(hk[2 * u]), bfhi(hk[2 * u])}, r1 = {bflo(hk[2 * u + 1]), bfhi(hk[2 * u + 1])};
#pragma unroll
                for (int k = 0; k < 4; ++k) {
                    const v2u p = *lp<v2u>(ra + (k < 3 ? k * 20480 : 122880) + u * 1024);
                    r0 += f32x2{bflo(p.x), bfhi(p.x)}; r1 += f32x2{bflo(p.y), bfhi(p.y)};
                }
                y[2 * u] = pk2v(r0); y[2 * u + 1] = pk2v(r1);
            }
            const rsrc_t ro = mk_rsrc(out, SEQ * DM * 2);
            const unsigned o = ((64 * t + 16 * hw + l15) * 640 + 80 * c + 20 * lg) * 2;
            __builtin_amdgcn_raw_buffer_store_b128(v4u{y[0], y[1], y[2], y[3]}, ro, o, 0, 0);
            __builtin_amdgcn_raw_buffer_store_b128(v4u{y[4], y[5], y[6], y[7]}, ro, o + 16, 0, 0);
            __builtin_amdgcn_raw_buffer_store_b64(v2u{y[8], y[9]}, ro, o + 32, 0, 0);
        }
        STAMP(19);
    }
}

}

extern "C" void kernel_launch(void* const* d_in, const int* in_sizes, int n_in, void* d_out, int out_size, void* d_ws, size_t ws_size,
                              hipStream_t stream) {
    (void)in_sizes; (void)n_in; (void)out_size; (void)ws_size;
    fused_block<<<256, 512, 0, stream>>>((const u16*)d_in[0], (const u16*)d_in[1], (const u16*)d_in[2], (const u16*)d_in[3],
                                         (const u16*)d_in[5], (const u16*)d_in[6], (const u16*)d_in[7], (u16*)d_out, (char*)d_ws);
}
```
